```python
import torch

def _softmax(x, axis=-1):
    return torch.softmax(x, dim=axis)
def _gelu(x):
    return torch.nn.functional.gelu(x, approximate="tanh")
def _silu(x):
    return torch.nn.functional.silu(x)
def _sigmoid(x):
    return torch.sigmoid(x)
def _layernorm(x, axis=-1, eps=1e-5):
    m = x.mean(dim=axis, keepdim=True)
    v = x.var(dim=axis, keepdim=True, unbiased=False)
    return (x - m) / torch.sqrt(v + eps)
def _scatter_add(tgt, idx, src, axis=0):
    return tgt.clone().index_add_(axis, idx, src)
def _segment_sum(src, seg, n):
    out = torch.zeros((n,) + src.shape[1:], dtype=src.dtype)
    return out.index_add_(0, seg, src)
def _pad(x, pw):
    flat = tuple(v for pair in reversed(pw) for v in pair)
    return torch.nn.functional.pad(x, flat)

kernel_name = 'gated_mix__ssm_conv_s4__ssm_conv_s4d__prompt'

def setup_inputs():

    _w = (lambda a: (lambda t: t.to(torch.bfloat16) if t.is_floating_point() else t)(torch.as_tensor(a)))
    def _A_setup():
        B, L, D, N = 1, 2048, 640, 192
        A_re = -(lambda _x: torch.exp(torch.as_tensor(_x)))((lambda *s: torch.randn(*s, dtype=torch.bfloat16) * 6.4)(D, N))
        A_im = torch.pi * (lambda *s: torch.randn(*s, dtype=torch.bfloat16) * 0.05)(D, N)
        B_re = (lambda *s: torch.randn(*s, dtype=torch.bfloat16) * 0.05)(D, N)
        B_im = (lambda *s: torch.randn(*s, dtype=torch.bfloat16) * 0.05)(D, N)
        C_re = (lambda *s: torch.randn(*s, dtype=torch.bfloat16) * 0.05)(D, N)
        C_im = (lambda *s: torch.randn(*s, dtype=torch.bfloat16) * 0.05)(D, N)
        log_dt = (lambda *s: torch.randn(*s, dtype=torch.bfloat16) * 0.05)(D) * 0.5 - 4.0
        Dskip = (lambda *s: torch.randn(*s, dtype=torch.bfloat16) * 0.05)(D)
        return {
            "u": _w((lambda *s: torch.randn(*s, dtype=torch.bfloat16) * 0.05)(B, L, D)),
            "A_re": _w(A_re),
            "A_im": _w(A_im),
            "B_re": _w(B_re),
            "B_im": _w(B_im),
            "C_re": _w(C_re),
            "C_im": _w(C_im),
            "log_dt": _w(log_dt),
            "Dskip": _w(Dskip),
        }
    _A_in = _A_setup()

    def _B_setup():
        Bsz, L, D, N = 1, 2048, 640, 192
        return {
            "u": _w((lambda *s: torch.randn(*s, dtype=torch.bfloat16) * 0.05)(Bsz, L, D)),
            "log_A_real": _w((lambda *s: torch.randn(*s, dtype=torch.bfloat16) * 6.4)(D, N) * 0.5),
            "A_imag": _w((lambda *s: torch.randn(*s, dtype=torch.bfloat16) * 0.05)(D, N) * 3.14),
            "C_real": _w((lambda *s: torch.randn(*s, dtype=torch.bfloat16) * 0.05)(D, N) / N ** 0.5),
            "C_imag": _w((lambda *s: torch.randn(*s, dtype=torch.bfloat16) * 0.05)(D, N) / N ** 0.5),
            "log_dt": _w((lambda *s: torch.randn(*s, dtype=torch.bfloat16) * 0.05)(D) * 0.5 - 4.0),
            "D_skip": _w((lambda *s: torch.randn(*s, dtype=torch.bfloat16) * 0.05)(D)),
        }
    _B_in = _B_setup()

    return {
        "A_u": _A_in["u"],
        "A_A_re": _A_in["A_re"],
        "A_A_im": _A_in["A_im"],
        "A_B_re": _A_in["B_re"],
        "A_B_im": _A_in["B_im"],
        "A_C_re": _A_in["C_re"],
        "A_C_im": _A_in["C_im"],
        "A_log_dt": _A_in["log_dt"],
        "A_Dskip": _A_in["Dskip"],
        "B_u": _B_in["u"],
        "B_log_A_real": _B_in["log_A_real"],
        "B_A_imag": _B_in["A_imag"],
        "B_C_real": _B_in["C_real"],
        "B_C_imag": _B_in["C_imag"],
        "B_log_dt": _B_in["log_dt"],
        "B_D_skip": _B_in["D_skip"],
        "gate_w": _w((lambda *s: torch.randn(*s, dtype=torch.bfloat16) * 0.05)(640) * 16.0),
    }

def reference(A_u, A_A_re, A_A_im, A_B_re, A_B_im, A_C_re, A_C_im, A_log_dt, A_Dskip, B_u, B_log_A_real, B_A_imag, B_C_real, B_C_imag, B_log_dt, B_D_skip, gate_w):
    def _A_ref(u, A_re, A_im, B_re, B_im, C_re, C_im, log_dt, Dskip):
        Bsz, L, D = u.shape
        dt = torch.exp(log_dt)[:, None]
        A = A_re + 1j * A_im
        Bc = B_re + 1j * B_im
        Cc = C_re + 1j * C_im
        logdA = dt * A
        dB = dt * Bc
        l = (lambda x, dt: x.to(dt))(torch.arange(L), A_re.dtype)
        P = torch.exp(logdA[:, :, None] * l[None, None, :])
        K = torch.einsum("dn,dnl,dn->dl", Cc, P, dB).real
        L2 = 2 * L
        Kp = _pad(K, ((0, 0), (0, L)))
        up = (lambda x, *d: x.permute(*d))(u, 0, 2, 1)
        up = _pad(up, ((0, 0), (0, 0), (0, L)))
        Kf = (lambda x, axes=(-2,-1): torch.fft.rfft2(x.float(), dim=axes))(Kp[:, None, :], axes=(-2, -1))
        Uf = (lambda x, axes=(-2,-1): torch.fft.rfft2(x.float(), dim=axes))(up[:, :, None, :], axes=(-2, -1))
        Yf = Uf * Kf[None, :, :, :]
        y = (lambda x, s, axes=(-2,-1): torch.fft.irfft2(x, s=s, dim=axes).to(torch.bfloat16))(Yf, (1, L2), axes=(-2, -1))[:, :, 0, :L]
        y = (lambda x, *d: x.permute(*d))(y, 0, 2, 1) + u * Dskip[None, None, :]
        return y

    def _B_ref(u, log_A_real, A_imag, C_real, C_imag, log_dt, D_skip):
        L = u.shape[1]
        dt = torch.exp(log_dt)
        A = -torch.exp(log_A_real) + 1j * A_imag
        C = (C_real + 1j * C_imag) * dt[:, None]
        pos = (lambda x, dt: x.to(dt))(torch.arange(L), log_dt.dtype)
        P = torch.exp((A * dt[:, None])[:, :, None] * pos[None, None, :])
        K = 2.0 * torch.einsum("dn,dnl->dl", C, P).real
        L2 = 2 * L
        u_t = (lambda x, *d: x.permute(*d))(u, 0, 2, 1)
        Kf = (lambda x, *a, **k: torch.fft.rfft(x.float(), *a, **k))(K, L2)
        Uf = (lambda x, *a, **k: torch.fft.rfft(x.float(), *a, **k))(u_t, L2)
        y = (lambda x, *a, **k: torch.fft.irfft(x, *a, **k).to(torch.bfloat16))(Uf * Kf[None, :, :], L2)[:, :, :L]
        y = (lambda x, *d: x.permute(*d))(y, 0, 2, 1)
        return y + u * D_skip[None, None, :]

    x = A_u
    g = _sigmoid((x * gate_w).sum(-1, keepdims=True))
    a = _A_ref(x, A_A_re, A_A_im, A_B_re, A_B_im, A_C_re, A_C_im, A_log_dt, A_Dskip)
    b = _B_ref(x, B_log_A_real, B_A_imag, B_C_real, B_C_imag, B_log_dt, B_D_skip)
    return g * a + (1 - g) * b
```

```cpp
#include <hip/hip_runtime.h>
#include <hip/hip_bf16.h>
#include <stdint.h>

constexpr int L = 2048, D = 640, N = 192;
typedef unsigned short u16;

__device__ __forceinline__ float bf2f(u16 x) { return __uint_as_float(((unsigned)x) << 16); }
__device__ __forceinline__ float rbf(float x) {
    unsigned u = __float_as_uint(x);
    u = (u + 0x7FFF + ((u >> 16) & 1)) & 0xFFFF0000u;
    return __uint_as_float(u);
}

__global__ void kgen(const u16* A_re, const u16* A_im, const u16* B_re, const u16* B_im, const u16* C_re, const u16* C_im,
                     const u16* A_logdt, const u16* logA, const u16* Aimag, const u16* Cr, const u16* Ci, const u16* B_logdt,
                     float* K) {
    int d = blockIdx.x; int ssm = blockIdx.y;
    __shared__ float sa[N], sb[N], scr[N], sci[N];
    if (threadIdx.x < N) {
        int n = threadIdx.x; int i = d * N + n;
        if (ssm == 0) {
            float dt = rbf(expf(bf2f(A_logdt[d])));
            sa[n] = dt * bf2f(A_re[i]); sb[n] = dt * bf2f(A_im[i]);
            float br = dt * bf2f(B_re[i]), bi = dt * bf2f(B_im[i]);
            float cr = bf2f(C_re[i]), ci = bf2f(C_im[i]);
            scr[n] = cr * br - ci * bi; sci[n] = cr * bi + ci * br;
        } else {
            float dt = rbf(expf(bf2f(B_logdt[d])));
            sa[n] = -rbf(expf(bf2f(logA[i]))) * dt; sb[n] = dt * bf2f(Aimag[i]);
            scr[n] = 2.f * dt * bf2f(Cr[i]); sci[n] = 2.f * dt * bf2f(Ci[i]);
        }
    }
    __syncthreads();
    for (int l = threadIdx.x; l < L; l += blockDim.x) {
        float p = rbf((float)l);
        float acc = 0.f;
        for (int n = 0; n < N; ++n) {
            float e = expf(sa[n] * p);
            float s, c; sincosf(sb[n] * p, &s, &c);
            acc += e * (scr[n] * c - sci[n] * s);
        }
        K[((size_t)ssm * D + d) * L + l] = acc;
    }
}

__global__ void gate(const u16* x, const u16* w, float* g) {
    int l = blockIdx.x;
    float s = 0.f;
    for (int d = threadIdx.x; d < D; d += 64) s += rbf(bf2f(x[l * D + d]) * bf2f(w[d]));
    for (int o = 32; o > 0; o >>= 1) s += __shfl_xor(s, o);
    if (threadIdx.x == 0) { float z = rbf(s); g[l] = rbf(1.f / (1.f + expf(-z))); }
}

__global__ void conv(const u16* x, const float* K, const float* g, const u16* DA, const u16* DB, u16* out) {
    int d = blockIdx.x;
    __shared__ float su[L], ska[L], skb[L];
    for (int l = threadIdx.x; l < L; l += blockDim.x) {
        su[l] = bf2f(x[l * D + d]); ska[l] = K[(size_t)d * L + l]; skb[l] = K[((size_t)D + d) * L + l];
    }
    __syncthreads();
    float da = bf2f(DA[d]), db = bf2f(DB[d]);
    for (int l = threadIdx.x; l < L; l += blockDim.x) {
        float ya = 0.f, yb = 0.f;
        for (int j = 0; j <= l; ++j) { float u = su[l - j]; ya += ska[j] * u; yb += skb[j] * u; }
        float u = su[l];
        float a = rbf(rbf(ya) + rbf(u * da));
        float b = rbf(rbf(yb) + rbf(u * db));
        float gg = g[l];
        float o = rbf(rbf(gg * a) + rbf(rbf(1.f - gg) * b));
        out[l * D + d] = (u16)(__float_as_uint(o) >> 16);
    }
}

extern "C" void kernel_launch(void* const* d_in, const int* in_sizes, int n_in,
                              void* d_out, int out_size, void* d_ws, size_t ws_size,
                              hipStream_t stream) {
    const u16* x = (const u16*)d_in[0];
    float* K = (float*)d_ws; float* g = K + 2 * D * L;
    kgen<<<dim3(D, 2), 256, 0, stream>>>((const u16*)d_in[1], (const u16*)d_in[2], (const u16*)d_in[3], (const u16*)d_in[4],
        (const u16*)d_in[5], (const u16*)d_in[6], (const u16*)d_in[7], (const u16*)d_in[10], (const u16*)d_in[11],
        (const u16*)d_in[12], (const u16*)d_in[13], (const u16*)d_in[14], K);
    gate<<<L, 64, 0, stream>>>(x, (const u16*)d_in[16], g);
    conv<<<D, 256, 0, stream>>>(x, K, g, (const u16*)d_in[8], (const u16*)d_in[15], (u16*)d_out);
}
```

```cpp
#include <hip/hip_runtime.h>
#include <stdint.h>

typedef unsigned short u16;
typedef unsigned long long u64;
typedef float f2 __attribute__((ext_vector_type(2)));
typedef float f4 __attribute__((ext_vector_type(4)));
typedef float f16v __attribute__((ext_vector_type(16)));
typedef _Float16 h2 __attribute__((ext_vector_type(2)));
typedef _Float16 h8 __attribute__((ext_vector_type(8)));
typedef __bf16 b2 __attribute__((ext_vector_type(2)));
typedef __bf16 b8 __attribute__((ext_vector_type(8)));
typedef unsigned u2 __attribute__((ext_vector_type(2)));
typedef unsigned u3 __attribute__((ext_vector_type(3), aligned(4)));
typedef unsigned u4 __attribute__((ext_vector_type(4)));
typedef short s4 __attribute__((ext_vector_type(4)));
#define LDS3 __attribute__((address_space(3)))
#define GLB1 __attribute__((address_space(1)))
#define CST4 __attribute__((address_space(4)))
#define DEV __device__ __forceinline__
#define PIN() __builtin_amdgcn_sched_barrier(0)

constexpr int L = 2048, D = 640, NM = 192;
constexpr int NMAIN = 160, NGATE = 64, NWG = NMAIN + NGATE, NT = 768;
constexpr int TB_W = 8704, ROW = 272;
constexpr int KA = 8 * TB_W, KA_W = 5760;
constexpr int X0 = 0, X1 = 1216, X2 = 2432, X3 = 3656, T3O = 4800, T4O = 5312;
constexpr int UO = KA + 8 * KA_W, U_C = 4576;
constexpr int RAW = UO + 4 * U_C;
constexpr int FLG = RAW + 16384;
constexpr int LDS_TOTAL = FLG + 80;
constexpr int OS_T = 768;
constexpr int WS_START = 256, WS_G = 4096;
constexpr unsigned SPIN_CAP = 1u << 17;

struct Params {
    const u16 *x, *Are, *Aim, *Bre, *Bim, *Cre, *Cim, *Aldt, *AD, *lA, *Aimag, *Cr, *Ci, *Bldt, *BD, *gw;
    u16* out;
    char* ws;
};

DEV float bf2f(unsigned v) { return __uint_as_float(v << 16); }
DEV float bfhi(unsigned v) { return __uint_as_float(v & 0xffff0000u); }
DEV unsigned pkbf(float lo, float hi) { f2 v = {lo, hi}; return __builtin_bit_cast(unsigned, __builtin_convertvector(v, b2)); }
DEV float rbf(float v) { return bfhi(pkbf(v, v)); }
DEV h2 toh(f2 v) { return __builtin_convertvector(v, h2); }
DEV f2 cmul(f2 x, f2 z) {
    f2 t = x * z.xx, r;
    asm("v_pk_fma_f32 %0, %1, %2, %3 op_sel:[1,1,0] op_sel_hi:[0,1,1] neg_lo:[1,0,0]" : "=v"(r) : "v"(x), "v"(z), "v"(t));
    return r;
}
DEV h2 cmulh(h2 x, h2 z) { h2 t = x * z.xx; h2 xs = {-x.y, x.x}; return __builtin_elementwise_fma(xs, z.yy, t); }
DEV h2 cmulhc(h2 x, h2 z) { h2 t = x * z.xx; h2 xs = {x.y, -x.x}; return __builtin_elementwise_fma(xs, z.yy, t); }
template <int R> DEV void putrow(h2 v) { asm volatile("ds_write_addtid_b32 %0 offset:%1" ::"v"(v), "i"(R * ROW) : "memory"); }
template <typename T> DEV T ldsr(unsigned a) { return *(LDS3 T*)a; }
template <typename T> DEV T ldsrv(unsigned a) { return *(volatile LDS3 T*)a; }
template <typename T> DEV void ldsw(unsigned a, T v) { *(LDS3 T*)a = v; }
template <typename T> DEV void ldswv(unsigned a, T v) { *(volatile LDS3 T*)a = v; }
DEV b8 frag64(unsigned a) { u2 p = ldsrv<u2>(a), q = ldsrv<u2>(a + 8); u4 r = {p.x, p.y, q.x, q.y}; return __builtin_bit_cast(b8, r); }
DEV u2 ldstr(unsigned a) { return __builtin_bit_cast(u2, __builtin_amdgcn_ds_read_tr16_b64_v4i16((LDS3 s4*)a)); }
DEV f4 mma(b8 a, b8 b, f4 c) { return __builtin_amdgcn_mfma_f32_16x16x32_bf16(a, b, c, 0, 0, 0); }
DEV float dot2(unsigned a, unsigned b, float c) { return __builtin_amdgcn_fdot2_f32_bf16(__builtin_bit_cast(b2, a), __builtin_bit_cast(b2, b), c, false); }
DEV u64 ld_agent(const char* p) { return __hip_atomic_load((GLB1 u64*)p, __ATOMIC_RELAXED, __HIP_MEMORY_SCOPE_AGENT); }
DEV void st_agent(char* p, u64 v) { __hip_atomic_store((GLB1 u64*)p, v, __ATOMIC_RELAXED, __HIP_MEMORY_SCOPE_AGENT); }
DEV u64 read_tag(const char* ws) {
    u64 e = *(const CST4 u64*)ws;
    asm volatile("" : "+s"(e));
    return e + 1;
}
DEV u4 band(int lane, int first, int width, unsigned val) {
    const unsigned m = ((((1u << width) - 1u) << (first + (lane & 15))) >> (8 * (lane >> 4))) & 0xffu;
    const unsigned t = m | (m << 15);
    u4 r;
#pragma unroll
    for (int i = 0; i < 4; ++i) r[i] = __umul24((t >> (2 * i)) & 0x10001u, val);
    asm volatile("" : "+v"(r));
    return r;
}
DEV void spread(u3 d, float (&P)[3], float (&Q)[3]) {
    const unsigned m[6] = {d.x << 16, d.x & 0xffff0000u, d.y << 16, d.y & 0xffff0000u, d.z << 16, d.z & 0xffff0000u};
#pragma unroll
    for (int j = 0; j < 3; ++j) {
        const auto r = __builtin_amdgcn_permlane32_swap(m[j], m[j + 3], false, false);
        P[j] = __uint_as_float(r[0]);
        Q[j] = __uint_as_float(r[1]);
    }
}
#define STAMP_AT(i)
#define STAMP_OUT()

DEV void gate_block(const Params& p, int gb) {
    const int tid = threadIdx.x, lane = tid & 63;
    const int row = gb * 32 + (tid >> 4), j = tid & 15;
    const u4* xr = (const u4*)(p.x + row * D);
    const u4* wr = (const u4*)p.gw;
    u4 xv[5], wv[5];
#pragma unroll
    for (int i = 0; i < 5; ++i) { xv[i] = xr[j + 16 * i]; wv[i] = wr[j + 16 * i]; }
    PIN();
    const u64 tag = read_tag(p.ws);
    if (tid == 0) st_agent(p.ws + WS_START + 8 * (NMAIN + gb), tag);
    float s = 0.f;
#pragma unroll
    for (int i = 0; i < 5; ++i)
#pragma unroll
        for (int e = 0; e < 4; ++e) {
            const unsigned q = pkbf(bf2f(xv[i][e]) * bf2f(wv[i][e]), bfhi(xv[i][e]) * bfhi(wv[i][e]));
            s += bf2f(q);
            s += bfhi(q);
        }
#pragma unroll
    for (int o = 8; o > 0; o >>= 1) s += __shfl_xor(s, o);
    const float z = rbf(s);
    const float g = 1.f / (1.f + expf(-z));
    if (j == 0) st_agent(p.ws + WS_G + 8 * row, (tag << 16) | (pkbf(g, g) & 0xffffu));
    if (gb == NGATE - 1 && tid < 64) {
#pragma unroll 1
        for (unsigned spin = 0; spin < SPIN_CAP; ++spin) {
            bool ok = true;
#pragma unroll
            for (int i = 0; i < 4; ++i) {
                const int e = min(lane + 64 * i, NWG - 1);
                ok &= ld_agent(p.ws + WS_START + 8 * e) == tag;
            }
            if (__all(ok)) break;
            __builtin_amdgcn_s_sleep(8);
        }
        if (lane == 0) st_agent(p.ws, tag);
    }
}

__global__ void __launch_bounds__(NT, 3) ssm_mix_kernel(Params p) {
    __shared__ __attribute__((aligned(16))) char smem[LDS_TOTAL];
    const int tid = threadIdx.x, lane = tid & 63;
    const int wv = __builtin_amdgcn_readfirstlane(tid >> 6);
    const int blk = blockIdx.x;
    if (blk >= NMAIN) {
        if (wv < 8) gate_block(p, blk - NMAIN);
        STAMP_AT(1);
        STAMP_OUT();
        return;
    }
    const unsigned sb = (unsigned)(size_t)(LDS3 char*)smem;
    const int i0 = blk >> 3, grp = (blk & 7) * 20 + i0;

    if (wv >= 8) {
        const int k = wv - 8;
        if (k == 0 && lane < 20) ldsw<unsigned>(sb + FLG + 4 * lane, 0u);
        if (lane < 30) ldsw<u4>(sb + UO + k * U_C + 16 * lane, u4{0, 0, 0, 0});
        const auto rx = __builtin_amdgcn_make_buffer_rsrc((void*)p.x, 0, L * D * 2, 0x00020000);
        const int r0 = 64 * k + ((i0 * 102) & ~63);
        asm volatile("s_waitcnt lgkmcnt(0)\n\ts_barrier" ::: "memory");
        STAMP_AT(1);
        u2 xv[8];
#pragma unroll
        for (int i = 0; i < 8; ++i) xv[i] = __builtin_amdgcn_raw_buffer_load_b64(rx, (((r0 + 256 * i) & (L - 1)) + lane) * (D * 2) + grp * 8, 0, 0);
        PIN();
        STAMP_AT(2);
        const u64 tag = read_tag(p.ws);
        if (k == 0 && lane == 0) st_agent(p.ws + WS_START + 8 * blk, tag);
        const unsigned raw = sb + RAW + 4096 * k;
        const unsigned ta = raw + 8 * ((lane & 48) + 4 * (lane & 3) + ((lane >> 2) & 3));
        const unsigned ua = sb + UO + (lane & 3) * U_C + 2 * (240 + (lane & 60));
#pragma unroll
        for (int i = 0; i < 8; i += 2) {
            ldswv<u2>(raw + 512 * i + 8 * lane, xv[i]);
            ldswv<u2>(raw + 512 * i + 512 + 8 * lane, xv[i + 1]);
            asm volatile("" ::: "memory");
            const u2 t0 = ldstr(ta + 512 * i), t1 = ldstr(ta + 512 * i + 512);
            ldswv<u2>(ua + 2 * ((r0 + 256 * i) & (L - 1)), t0);
            ldswv<u2>(ua + 2 * ((r0 + 256 * i + 256) & (L - 1)), t1);
        }
        ldswv<unsigned char>(sb + FLG + 64 + k, 1);
        STAMP_AT(3);
        const char* gp = p.ws + WS_G + 8 * (64 * k + lane);
        const u64 want = tag << 16;
        u64 gr[8];
#pragma unroll 1
        for (unsigned spin = 0; spin < SPIN_CAP; ++spin) {
            u64 df = 0;
#pragma unroll
            for (int T = 0; T < 8; ++T) gr[T] = ld_agent(gp + 2048 * T);
#pragma unroll
            for (int T = 0; T < 8; ++T) df |= gr[T] ^ want;
            if (__builtin_amdgcn_ballot_w64(((unsigned)(df >> 32) | ((unsigned)df >> 16)) != 0) == 0) break;
            __builtin_amdgcn_s_sleep(4);
        }
        unsigned gw[8];
#pragma unroll
        for (int T = 0; T < 8; ++T) { const float g = bf2f((unsigned)gr[T]); gw[T] = pkbf(g, 1.f - g); }
        STAMP_AT(4);
        const int rr = (lane >> 2) & 3;
        const unsigned oa = sb + ((rr >> 1) + 4 * (rr & 1)) * TB_W + 16 * rr + 8 * (4 * k + (lane >> 4) + 24 * (lane & 3));
        const auto ro = __builtin_amdgcn_make_buffer_rsrc((void*)p.out, 0, L * D * 2, 0x00020000);
        const int ov = (64 * k + lane) * (D * 2) + grp * 8;
#pragma unroll
        for (int T = 0; T < 8; ++T) {
#pragma unroll 1
            for (unsigned spin = 0; spin < SPIN_CAP; ++spin) {
                const u2 fl = ldsrv<u2>(sb + FLG + 8 * T);
                if (__builtin_amdgcn_readfirstlane(fl.x & fl.y) == 0x01010101u) break;
                __builtin_amdgcn_s_sleep(1);
            }
            asm volatile("" ::: "memory");
            const u2 a = ldstr(oa + T * OS_T), b = ldstr(oa + T * OS_T + 2 * TB_W);
            const float o0 = dot2(a.x, gw[T], 0.f), o1 = dot2(a.y, gw[T], 0.f), o2 = dot2(b.x, gw[T], 0.f), o3 = dot2(b.y, gw[T], 0.f);
            __builtin_amdgcn_raw_buffer_store_b64(u2{pkbf(o0, o1), pkbf(o2, o3)}, ro, ov, T * 256 * D * 2, 0);
        }
        return;
    }

    const int c = wv & 3, s = wv >> 2, ch = grp * 4 + c;
    u3 d0, d1, d2;
    {
        const bool up = lane >= 32;
        const u16 *p0 = s ? p.lA : p.Are, *p1 = s ? p.Aimag : p.Aim, *p2 = s ? p.Cr : p.Cre, *p3 = s ? p.Ci : p.Cim;
        const u16 *p4 = s ? p.Cr : p.Bre, *p5 = s ? p.Ci : p.Bim;
        const int o = ch * NM + 6 * (lane & 31);
        d0 = *(const u3*)((up ? p1 : p0) + o);
        d1 = *(const u3*)((up ? p3 : p2) + o);
        d2 = *(const u3*)((up ? p5 : p4) + o);
    }
    const u64 ldt4 = ((const CST4 u64*)(s ? p.Bldt : p.Aldt))[grp], dsk4 = ((const CST4 u64*)(s ? p.BD : p.AD))[grp];
    PIN();
    asm volatile("s_barrier" ::: "memory");

    const unsigned tb = sb + wv * TB_W, ka = sb + KA + wv * KA_W, ub = sb + UO + c * U_C;
    {
        const u4 z4 = {0, 0, 0, 0};
#pragma unroll
        for (int i = 0; i < 5; ++i) ldsw<u4>(ka + 1024 * i + 16 * lane, z4);
        ldsw<u4>(ka + 4736 + 16 * lane, z4);
    }
    asm volatile("s_mov_b32 m0, %0\n\ts_nop 0" ::"s"(tb) : "memory");
    { h2 one = {(_Float16)1.f, (_Float16)0.f}; putrow<0>(one); }
    unsigned wra;
    {
        const int r = lane & 31;
        constexpr u64 t0 = 2ull | 3ull << 5 | 4ull << 10 | 5ull << 15 | 12ull << 20 | 12ull << 25 | 13ull << 30 | 1ull << 35 | 14ull << 40 | 15ull << 45 | 16ull << 50 | 18ull << 55;
        constexpr u64 t1 = 1ull | 1ull << 5 | 6ull << 10 | 7ull << 15 | 19ull << 20 | 20ull << 25 | 21ull << 30 | 22ull << 35 | 8ull << 40 | 9ull << 45 | 10ull << 50 | 11ull << 55;
        constexpr u64 t2 = 2ull | 2ull << 5 | 2ull << 10 | 2ull << 15 | 12ull << 20 | 12ull << 25 | 12ull << 30 | 12ull << 35;
        const u64 tt = r < 12 ? t0 : r < 24 ? t1 : t2;
        const int rr = r < 12 ? r : r < 24 ? r - 12 : r - 24;
        wra = tb + ((int)(tt >> (5 * rr)) & 31) * ROW + 16 * (lane >> 5);
    }
    const unsigned vra = tb + (lane & 31) * ROW + 16 * (lane >> 5);
    const b8 B5 = __builtin_bit_cast(b8, band(lane, 6, 5, 0x3f80u)), B3a = __builtin_bit_cast(b8, band(lane, 3, 3, 0x3f80u));
    const b8 B3b = __builtin_bit_cast(b8, band(lane, 11, 3, 0x3f80u)), B9 = __builtin_bit_cast(b8, band(lane, 4, 9, 0x3f80u));
    const b8 B7 = __builtin_bit_cast(b8, band(lane, 5, 7, 0x3f80u));
    const u4 msp = band(lane, 12, 2, 0xffffu);
    const float dt = rbf(expf(bf2f((unsigned)(ldt4 >> (16 * c)) & 0xffffu)));
    const unsigned dlo = (unsigned)(dsk4 >> (16 * c)) & 0xffffu, dhi = dlo << 16;
    float ka2 = dt * 1.4426950408889634f, kf = dt * 0.15915494309189535f;
    asm volatile("" : "+v"(ka2), "+v"(kf), "+v"(wra));
    PIN();
    STAMP_AT(1);

    float pa[3], pf[3];
    f2 pc[3];
    {
        float cr[3], ci[3];
        spread(d0, pa, pf);
        spread(d1, cr, ci);
#pragma unroll
        for (int j = 0; j < 3; ++j) { pf[j] *= kf; pc[j] = f2{cr[j], ci[j]}; }
        if (s == 0) {
            spread(d2, cr, ci);
#pragma unroll
            for (int j = 0; j < 3; ++j) pc[j] = cmul(pc[j], f2{cr[j], ci[j]});
        } else {
#pragma unroll
            for (int j = 0; j < 3; ++j) pa[j] = -rbf(__builtin_amdgcn_exp2f(pa[j] * 1.4426950408889634f));
        }
    }
    float mx = 0.f;
#pragma unroll
    for (int j = 0; j < 3; ++j) { pa[j] *= ka2; mx = fmaxf(mx, fmaxf(fabsf(pc[j].x), fabsf(pc[j].y))); }
    unsigned mi = __float_as_uint(mx);
    mi = max(mi, (unsigned)__builtin_amdgcn_update_dpp(0, (int)mi, 0x128, 0xf, 0xf, false));
    mi = max(mi, (unsigned)__builtin_amdgcn_update_dpp(0, (int)mi, 0x124, 0xf, 0xf, false));
    mi = max(mi, (unsigned)__builtin_amdgcn_update_dpp(0, (int)mi, 0x122, 0xf, 0xf, false));
    mi = max(mi, (unsigned)__builtin_amdgcn_update_dpp(0, (int)mi, 0x121, 0xf, 0xf, false));
    const unsigned ms = max(max((unsigned)__builtin_amdgcn_readlane(mi, 0), (unsigned)__builtin_amdgcn_readlane(mi, 16)),
                            max((unsigned)__builtin_amdgcn_readlane(mi, 32), (unsigned)__builtin_amdgcn_readlane(mi, 48)));
    const unsigned eb = min(max(ms >> 23, 20u), 240u);
    const float scale = __uint_as_float((267u - eb) << 23);
    const float inv = (s == 0 ? dt : 2.f * dt) * __uint_as_float((eb - 13u) << 23);
    STAMP_AT(2);

    f16v kacc = {};
#pragma unroll 1
    for (int j = 0; j < 3; ++j) {
        const float a = pa[0], f = pf[0];
        const f2 cs = pc[0] * scale;
        pa[0] = pa[1]; pa[1] = pa[2]; pf[0] = pf[1]; pf[1] = pf[2]; pc[0] = pc[1]; pc[1] = pc[2];
        const float t8 = __builtin_amdgcn_fractf(f * 8.f), e8 = __builtin_amdgcn_exp2f(a * 8.f);
        const f2 v1 = f2{__builtin_amdgcn_cosf(t8), -__builtin_amdgcn_sinf(t8)} * e8;
        const float t1 = __builtin_amdgcn_fractf(f), e1 = __builtin_amdgcn_exp2f(a);
        const f2 z1 = f2{__builtin_amdgcn_cosf(t1), __builtin_amdgcn_sinf(t1)} * e1;
        const f2 v2 = cmul(v1, v1), v3 = cmul(v2, v1), v4 = cmul(v2, v2), v8 = cmul(v4, v4), v12 = cmul(v8, v4), v16 = cmul(v8, v8);
        const f2 v20 = cmul(v16, v4), v24 = cmul(v16, v8), v28 = cmul(v16, v12);
        const f2 y256 = cmul(v16, v16), y512 = cmul(y256, y256), y1024 = cmul(y512, y512);
        const h2 P1 = toh(v1), P2 = toh(v2), P3 = toh(v3);
        putrow<1>(P1); putrow<2>(P2); putrow<3>(P3);
#define VROW(B, V) { const h2 Q = toh(V); putrow<B>(Q); putrow<B + 1>(cmulh(Q, P1)); putrow<B + 2>(cmulh(Q, P2)); putrow<B + 3>(cmulh(Q, P3)); }
        VROW(4, v4) VROW(8, v8) VROW(12, v12) VROW(16, v16) VROW(20, v20) VROW(24, v24) VROW(28, v28)
#undef VROW
        h8 vf[8];
#pragma unroll
        for (int q = 0; q < 8; ++q) vf[q] = ldsr<h8>(vra + 32 * q);
        const f2 w1 = cmul(cs, z1), w2 = cmul(w1, z1), w3 = cmul(w2, z1), w4 = cmul(w3, z1);
        const f2 w5 = cmul(w4, z1), w6 = cmul(w5, z1), w7 = cmul(w6, z1), w8 = cmul(w7, z1);
        const h2 W0 = toh(cs), W2 = toh(w2), W4 = toh(w4), W6 = toh(w6), W8 = toh(w8);
        const h2 Y256 = toh(y256), Y512 = toh(y512), Y1024 = toh(y1024);
        putrow<2>(W0); putrow<3>(toh(w1)); putrow<4>(W2); putrow<5>(toh(w3));
        putrow<14>(W4); putrow<15>(toh(w5)); putrow<16>(W6); putrow<18>(toh(w7));
        putrow<12>(cmulhc(W0, Y256)); putrow<13>(cmulhc(W2, Y256)); putrow<1>(cmulhc(W4, Y256)); putrow<6>(cmulhc(W6, Y256)); putrow<7>(cmulhc(W8, Y256));
        const h2 r516 = cmulhc(W4, Y512), r520 = cmulhc(W8, Y512), r1032 = cmulhc(W8, Y1024), r1544 = cmulhc(r1032, Y512);
        putrow<20>(r516); putrow<19>(r520); putrow<9>(cmulhc(r516, Y256)); putrow<8>(cmulhc(r520, Y256));
        putrow<21>(r1032); putrow<22>(cmulhc(r1032, Y256)); putrow<10>(r1544); putrow<11>(cmulhc(r1544, Y256));
        h8 wf[8];
#pragma unroll
        for (int q = 0; q < 8; ++q) wf[q] = ldsr<h8>(wra + 32 * q);
        PIN();
#pragma unroll
        for (int q = 0; q < 8; ++q) kacc = __builtin_amdgcn_mfma_f32_32x32x16_f16(wf[q], vf[q], kacc, 0, 0, 0);
        asm volatile("" ::: "memory");
    }
    STAMP_AT(3);

#pragma unroll 1
    for (unsigned spin = 0; spin < SPIN_CAP; ++spin) {
        if (__builtin_amdgcn_readfirstlane(ldsrv<unsigned>(sb + FLG + 64)) == 0x01010101u) break;
        __builtin_amdgcn_s_sleep(1);
    }
    asm volatile("" ::: "memory");
    STAMP_AT(4);
    const int n = lane & 15, h = lane >> 4;
    const unsigned ga = ub + 32 * n + 16 * h;
    b8 A16[6], A24[6];
#pragma unroll
    for (int I = 0; I < 2; ++I) { A24[I] = ldsr<b8>(ga + 64 * (8 * I + 7) - 16); A16[I] = ldsr<b8>(ga + 64 * (8 * I + 7)); }
    PIN();

    b8 Fs;
    {
        float x[12];
#pragma unroll
        for (int i = 0; i < 12; ++i) x[i] = kacc[i] * inv;
        ldsw<unsigned>(ka + T3O + 4 * (95 - lane), pkbf(x[8], x[9]));
        const unsigned o4 = pkbf(x[10], x[11]);
        const unsigned t4a = ka + T4O + 2 * (127 - 64 * (lane >> 5) - (lane & 31));
        ldsw<u16>(t4a, (u16)o4);
        ldsw<u16>(t4a - 64, (u16)(o4 >> 16));
        Fs = __builtin_bit_cast(b8, msp & (unsigned)__builtin_amdgcn_readlane(pkbf(x[8], x[8]), 63));
        const unsigned D0 = pkbf(x[1], x[0]), D1 = pkbf(x[3], x[2]), D2 = pkbf(x[5], x[4]), D3 = pkbf(x[7], x[6]);
        const unsigned E0 = pkbf(x[2], x[1]), E1 = pkbf(x[4], x[3]), E2 = pkbf(x[6], x[5]), KK = pkbf(x[7], x[7]);
        const unsigned D4 = __builtin_amdgcn_update_dpp(KK, D0, 0x130, 0xf, 0xf, false);
        const unsigned E4 = __builtin_amdgcn_update_dpp(KK, E0, 0x130, 0xf, 0xf, false);
        const unsigned E3 = __builtin_amdgcn_alignbit(D3, D4, 16);
        const unsigned xa = ka + 2 * 536 - 16 * lane;
        ldsw<u4>(xa + X0, u4{E3, E2, E1, E0});
        ldsw<u4>(xa + X1, u4{D4, D3, D2, D1});
        ldsw<u4>(xa + X2, u4{E4, E3, E2, E1});
        ldsw<u4>(xa + X3 + 8, u4{D3, D2, D1, D0});
        if (lane == 0 || lane == 63) {
            const bool b = lane == 0;
            const unsigned KH = KK & 0xffff0000u, x0 = D0 >> 16;
            ldsw<u2>(ka + X0 + (b ? 1088 : 56), b ? u2{x0, 0} : u2{0, KK});
            ldsw<u2>(ka + X1 + (b ? 1088 : 56), b ? u2{D0, 0} : u2{0, KH});
            ldsw<u2>(ka + (b ? X2 + 1088 : X3 + 64), b ? u2{E0, x0} : u2{KH, KK});
        }
    }
    asm volatile("" ::: "memory");
    STAMP_AT(5);

    f4 acc[8];
    b8 F[17], G[64];
    const unsigned fa = ka + (n & 3) * 1216 + ((n & 3) == 3 ? 8 : 0) + 2 * (528 - 4 * (n >> 2) + 8 * h) - 1024;
    const f4 z = {0.f, 0.f, 0.f, 0.f};
    {
        b8 K3[3][2], K4[4];
        const unsigned k3a = ka + T3O + 2 * (64 + 252 - 64 * 4 - 4 * n + 16 * h);
        unsigned k4a = ka + T4O + 4 * (127 - 16 * 7 - n + 4 * h);
        asm volatile("" : "+v"(k4a));
#pragma unroll
        for (int d = 0; d < 4; ++d) {
            u4 r;
#pragma unroll
            for (int e = 0; e < 4; ++e) r[e] = ldsr<unsigned>(k4a + 64 * (3 - d) + 4 * e);
            K4[d] = __builtin_bit_cast(b8, r);
        }
#pragma unroll
        for (int d = 0; d < 3; ++d)
#pragma unroll
            for (int e = 0; e < 2; ++e) K3[d][e] = frag64(k3a + 128 * (2 - d) + 16 * e);
        PIN();
        f4 R[2][6];
        auto box = [&](int I) {
            f4* r = R[I & 1];
            if (I < 4) { r[4] = mma(A24[I], B9, z); r[5] = mma(A16[I], B7, z); }
            r[0] = mma(A24[I], B5, z); r[1] = mma(A16[I], B3a, z); r[2] = mma(A16[I], B5, z); r[3] = mma(A16[I], B3b, z);
            if (I < 4) acc[I + 4] = mma(Fs, A16[I], I == 0 ? z : acc[I + 4]);
        };
        acc[2] = z; acc[3] = z; acc[5] = z; acc[6] = z; acc[7] = z;
        box(0);
        PIN();
#pragma unroll
        for (int I = 0; I < 6; ++I) {
            if (I < 5) box(I + 1);
            if (I < 4) { A24[I + 2] = ldsr<b8>(ga + 64 * (8 * I + 23) - 16); A16[I + 2] = ldsr<b8>(ga + 64 * (8 * I + 23)); }
            if (I == 4) {
#pragma unroll
                for (int t = 7; t >= 0; --t) F[t] = frag64(fa + 64 * (16 - t));
            }
            if (I == 5) {
#pragma unroll
                for (int w = 0; w < 8; ++w) G[w] = ldsr<b8>(ga + 64 * w);
            }
            PIN();
            const f4* r = R[I & 1];
            if (I < 4) {
                const u4 zz = {pkbf(r[4][0], r[5][0]), pkbf(r[4][1], r[5][1]), pkbf(r[4][2], r[5][2]), pkbf(r[4][3], r[5][3])};
#pragma unroll
                for (int d = 0; d < 4; ++d)
                    if (I + 4 + d < 8) acc[I + 4 + d] = mma(__builtin_bit_cast(b8, zz), K4[d], acc[I + 4 + d]);
            }
#pragma unroll
            for (int e = 0; e < 2; ++e) {
                const u4 zz = {pkbf(r[0][2 * e], r[1][2 * e]), pkbf(r[2][2 * e], r[3][2 * e]), pkbf(r[0][2 * e + 1], r[1][2 * e + 1]), pkbf(r[2][2 * e + 1], r[3][2 * e + 1])};
#pragma unroll
                for (int d = 0; d < 3; ++d)
                    if (I + 2 + d < 8) acc[I + 2 + d] = mma(__builtin_bit_cast(b8, zz), K3[d][e], acc[I + 2 + d]);
            }
            PIN();
        }
    }
    STAMP_AT(6);

    const unsigned ea = ub + 2 * (240 + 16 * n + 4 * h);
    const unsigned oa = tb + 16 * (2 * (c & 1) + s) + 8 * (n + 24 * h);
    const unsigned fl = sb + FLG + wv;
    u2 uu;
    auto finish = [&](int T) {
        const float p0 = dot2(uu.x, dlo, acc[T][0]), p1 = dot2(uu.x, dhi, acc[T][1]), p2 = dot2(uu.y, dlo, acc[T][2]), p3 = dot2(uu.y, dhi, acc[T][3]);
        ldswv<u2>(oa + T * OS_T, u2{pkbf(p0, p1), pkbf(p2, p3)});
        ldswv<unsigned char>(fl + 8 * T, 1);
    };
    acc[0] = z; acc[1] = z;
#pragma unroll
    for (int T = 0; T < 8; ++T) {
        const int t0 = T == 0 ? 7 : T == 1 ? 15 : 16;
#pragma unroll
        for (int t = t0; t >= 0; --t) {
            const int k = t0 - t;
            acc[T] = mma(F[t], G[8 * T - t + 7], acc[T]);
            if (T == 0) { F[15 - k] = frag64(fa + 64 * (1 + k)); if (k == 7) F[16] = frag64(fa); }
            if (T >= 1 && k < 8) { const int w = 8 * T + k; G[w] = ldsr<b8>(ga + 64 * w); }
            if (T >= 1 && k == 3) finish(T - 1);
            if (k == t0 - 2) uu = ldsrv<u2>(ea + 512 * T);
            PIN();
        }
        if (T == 0) STAMP_AT(7);
        if (T == 1) STAMP_AT(8);
        if (T == 3) STAMP_AT(9);
        if (T == 5) STAMP_AT(10);
    }
    STAMP_AT(11);
    finish(7);
    STAMP_AT(15);
    STAMP_OUT();
}

extern "C" void kernel_launch(void* const* d_in, const int* in_sizes, int n_in, void* d_out, int out_size, void* d_ws, size_t ws_size,
                              hipStream_t stream) {
    Params p;
    p.x = (const u16*)d_in[0];
    p.Are = (const u16*)d_in[1]; p.Aim = (const u16*)d_in[2]; p.Bre = (const u16*)d_in[3]; p.Bim = (const u16*)d_in[4];
    p.Cre = (const u16*)d_in[5]; p.Cim = (const u16*)d_in[6]; p.Aldt = (const u16*)d_in[7]; p.AD = (const u16*)d_in[8];
    p.lA = (const u16*)d_in[10]; p.Aimag = (const u16*)d_in[11]; p.Cr = (const u16*)d_in[12]; p.Ci = (const u16*)d_in[13];
    p.Bldt = (const u16*)d_in[14]; p.BD = (const u16*)d_in[15]; p.gw = (const u16*)d_in[16];
    p.out = (u16*)d_out;
    p.ws = (char*)d_ws;
    ssm_mix_kernel<<<NWG, NT, 0, stream>>>(p);
}
```

```cpp
#define STAMP 1
#include <hip/hip_runtime.h>
#include <stdint.h>

typedef unsigned short u16;
typedef unsigned long long u64;
typedef float f2 __attribute__((ext_vector_type(2)));
typedef float f4 __attribute__((ext_vector_type(4)));
typedef float f16v __attribute__((ext_vector_type(16)));
typedef _Float16 h2 __attribute__((ext_vector_type(2)));
typedef _Float16 h8 __attribute__((ext_vector_type(8)));
typedef __bf16 b2 __attribute__((ext_vector_type(2)));
typedef __bf16 b8 __attribute__((ext_vector_type(8)));
typedef unsigned u2 __attribute__((ext_vector_type(2)));
typedef unsigned u3 __attribute__((ext_vector_type(3), aligned(4)));
typedef unsigned u4 __attribute__((ext_vector_type(4)));
typedef short s4 __attribute__((ext_vector_type(4)));
#define LDS3 __attribute__((address_space(3)))
#define GLB1 __attribute__((address_space(1)))
#define CST4 __attribute__((address_space(4)))
#define DEV __device__ __forceinline__
#define PIN() __builtin_amdgcn_sched_barrier(0)

constexpr int L = 2048, D = 640, NM = 192;
constexpr int NMAIN = 160, NGATE = 64, NWG = NMAIN + NGATE, NT = 768;
constexpr int TB_W = 8704, ROW = 272;
constexpr int KA = 8 * TB_W, KA_W = 5760;
constexpr int X0 = 0, X1 = 1216, X2 = 2432, X3 = 3656, T3O = 4800, T4O = 5312;
constexpr int UO = KA + 8 * KA_W, U_C = 4576;
constexpr int RAW = UO + 4 * U_C;
constexpr int FLG = RAW + 16384;
constexpr int LDS_TOTAL = FLG + 80;
constexpr int OS_T = 768;
constexpr int WS_START = 256, WS_G = 4096;
constexpr unsigned SPIN_CAP = 1u << 17;

struct Params {
    const u16 *x, *Are, *Aim, *Bre, *Bim, *Cre, *Cim, *Aldt, *AD, *lA, *Aimag, *Cr, *Ci, *Bldt, *BD, *gw;
    u16* out;
    char* ws;
};

DEV float bf2f(unsigned v) { return __uint_as_float(v << 16); }
DEV float bfhi(unsigned v) { return __uint_as_float(v & 0xffff0000u); }
DEV unsigned pkbf(float lo, float hi) { f2 v = {lo, hi}; return __builtin_bit_cast(unsigned, __builtin_convertvector(v, b2)); }
DEV float rbf(float v) { return bfhi(pkbf(v, v)); }
DEV h2 toh(f2 v) { return __builtin_convertvector(v, h2); }
DEV f2 cmul(f2 x, f2 z) {
    f2 t = x * z.xx, r;
    asm("v_pk_fma_f32 %0, %1, %2, %3 op_sel:[1,1,0] op_sel_hi:[0,1,1] neg_lo:[1,0,0]" : "=v"(r) : "v"(x), "v"(z), "v"(t));
    return r;
}
DEV h2 cmulh(h2 x, h2 z) { h2 t = x * z.xx; h2 xs = {-x.y, x.x}; return __builtin_elementwise_fma(xs, z.yy, t); }
DEV h2 cmulhc(h2 x, h2 z) { h2 t = x * z.xx; h2 xs = {x.y, -x.x}; return __builtin_elementwise_fma(xs, z.yy, t); }
template <int R> DEV void putrow(h2 v) { asm volatile("ds_write_addtid_b32 %0 offset:%1" ::"v"(v), "i"(R * ROW) : "memory"); }
template <typename T> DEV T ldsr(unsigned a) { return *(LDS3 T*)a; }
template <typename T> DEV T ldsrv(unsigned a) { return *(volatile LDS3 T*)a; }
template <typename T> DEV void ldsw(unsigned a, T v) { *(LDS3 T*)a = v; }
template <typename T> DEV void ldswv(unsigned a, T v) { *(volatile LDS3 T*)a = v; }
DEV b8 frag64(unsigned a) { u2 p = ldsrv<u2>(a), q = ldsrv<u2>(a + 8); u4 r = {p.x, p.y, q.x, q.y}; return __builtin_bit_cast(b8, r); }
DEV u2 ldstr(unsigned a) { return __builtin_bit_cast(u2, __builtin_amdgcn_ds_read_tr16_b64_v4i16((LDS3 s4*)a)); }
DEV f4 mma(b8 a, b8 b, f4 c) { return __builtin_amdgcn_mfma_f32_16x16x32_bf16(a, b, c, 0, 0, 0); }
DEV float dot2(unsigned a, unsigned b, float c) { return __builtin_amdgcn_fdot2_f32_bf16(__builtin_bit_cast(b2, a), __builtin_bit_cast(b2, b), c, false); }
DEV u64 ld_agent(const char* p) { return __hip_atomic_load((GLB1 u64*)p, __ATOMIC_RELAXED, __HIP_MEMORY_SCOPE_AGENT); }
DEV void st_agent(char* p, u64 v) { __hip_atomic_store((GLB1 u64*)p, v, __ATOMIC_RELAXED, __HIP_MEMORY_SCOPE_AGENT); }
DEV u64 read_tag(const char* ws) {
    u64 e = *(const CST4 u64*)ws;
    asm volatile("" : "+s"(e));
    return e + 1;
}
DEV u4 band(int lane, int first, int width, unsigned val) {
    const unsigned m = ((((1u << width) - 1u) << (first + (lane & 15))) >> (8 * (lane >> 4))) & 0xffu;
    const unsigned t = m | (m << 15);
    u4 r;
#pragma unroll
    for (int i = 0; i < 4; ++i) r[i] = __umul24((t >> (2 * i)) & 0x10001u, val);
    asm volatile("" : "+v"(r));
    return r;
}
DEV void spread(u3 d, float (&P)[3], float (&Q)[3]) {
    const unsigned m[6] = {d.x << 16, d.x & 0xffff0000u, d.y << 16, d.y & 0xffff0000u, d.z << 16, d.z & 0xffff0000u};
#pragma unroll
    for (int j = 0; j < 3; ++j) {
        const auto r = __builtin_amdgcn_permlane32_swap(m[j], m[j + 3], false, false);
        P[j] = __uint_as_float(r[0]);
        Q[j] = __uint_as_float(r[1]);
    }
}
#define STAMP_AT(i) do { if ((i) == 99) { PIN(); dg_t = __builtin_amdgcn_s_memrealtime(); asm volatile("s_waitcnt lgkmcnt(0)" : "+s"(dg_t) :: "memory"); PIN(); } } while (0)
#define STAMP_OUT() do { if (wv == 8 && blk < NMAIN && (-1 < 0 || blk == -1)) { const u64 lim = rt0 + 3000 + (dg_t - dg_0) * 0; for (int q_ = 0; q_ < 2000000; ++q_) { if (__builtin_amdgcn_s_memrealtime() >= lim) break; __builtin_amdgcn_s_sleep(1); } } } while (0)

DEV void gate_block(const Params& p, int gb) {
    const int tid = threadIdx.x, lane = tid & 63;
    const int row = gb * 32 + (tid >> 4), j = tid & 15;
    const u4* xr = (const u4*)(p.x + row * D);
    const u4* wr = (const u4*)p.gw;
    u4 xv[5], wv[5];
#pragma unroll
    for (int i = 0; i < 5; ++i) { xv[i] = xr[j + 16 * i]; wv[i] = wr[j + 16 * i]; }
    PIN();
    const u64 tag = read_tag(p.ws);
    if (tid == 0) st_agent(p.ws + WS_START + 8 * (NMAIN + gb), tag);
    float s = 0.f;
#pragma unroll
    for (int i = 0; i < 5; ++i)
#pragma unroll
        for (int e = 0; e < 4; ++e) {
            const unsigned q = pkbf(bf2f(xv[i][e]) * bf2f(wv[i][e]), bfhi(xv[i][e]) * bfhi(wv[i][e]));
            s += bf2f(q);
            s += bfhi(q);
        }
#pragma unroll
    for (int o = 8; o > 0; o >>= 1) s += __shfl_xor(s, o);
    const float z = rbf(s);
    const float g = 1.f / (1.f + expf(-z));
    if (j == 0) st_agent(p.ws + WS_G + 8 * row, (tag << 16) | (pkbf(g, g) & 0xffffu));
    if (gb == NGATE - 1 && tid < 64) {
#pragma unroll 1
        for (unsigned spin = 0; spin < SPIN_CAP; ++spin) {
            bool ok = true;
#pragma unroll
            for (int i = 0; i < 4; ++i) {
                const int e = min(lane + 64 * i, NWG - 1);
                ok &= ld_agent(p.ws + WS_START + 8 * e) == tag;
            }
            if (__all(ok)) break;
            __builtin_amdgcn_s_sleep(8);
        }
        if (lane == 0) st_agent(p.ws, tag);
    }
}

__global__ void __launch_bounds__(NT, 3) ssm_mix_kernel(Params p) {
    __shared__ __attribute__((aligned(16))) char smem[LDS_TOTAL];
    const int tid = threadIdx.x, lane = tid & 63;
    const int wv = __builtin_amdgcn_readfirstlane(tid >> 6);
    const int blk = blockIdx.x;
    const u64 rt0 = __builtin_amdgcn_s_memrealtime();
    const u64 dg_0 = rt0;
    u64 dg_t = dg_0;
    if (blk >= NMAIN) {
        if (wv < 8) gate_block(p, blk - NMAIN);
        STAMP_AT(1);
        STAMP_OUT();
        return;
    }
    const unsigned sb = (unsigned)(size_t)(LDS3 char*)smem;
    const int i0 = blk >> 3, grp = (blk & 7) * 20 + i0;

    if (wv >= 8) {
        const int k = wv - 8;
        if (k == 0 && lane < 20) ldsw<unsigned>(sb + FLG + 4 * lane, 0u);
        if (lane < 30) ldsw<u4>(sb + UO + k * U_C + 16 * lane, u4{0, 0, 0, 0});
        const auto rx = __builtin_amdgcn_make_buffer_rsrc((void*)p.x, 0, L * D * 2, 0x00020000);
        const int r0 = 64 * k + ((i0 * 102) & ~63);
        asm volatile("s_waitcnt lgkmcnt(0)\n\ts_barrier" ::: "memory");
        STAMP_AT(1);
        u2 xv[8];
#pragma unroll
        for (int i = 0; i < 8; ++i) xv[i] = __builtin_amdgcn_raw_buffer_load_b64(rx, (((r0 + 256 * i) & (L - 1)) + lane) * (D * 2) + grp * 8, 0, 0);
        PIN();
        STAMP_AT(2);
        const u64 tag = read_tag(p.ws);
        if (k == 0 && lane == 0) st_agent(p.ws + WS_START + 8 * blk, tag);
        const unsigned raw = sb + RAW + 4096 * k;
        const unsigned ta = raw + 8 * ((lane & 48) + 4 * (lane & 3) + ((lane >> 2) & 3));
        const unsigned ua = sb + UO + (lane & 3) * U_C + 2 * (240 + (lane & 60));
#pragma unroll
        for (int i = 0; i < 8; i += 2) {
            ldswv<u2>(raw + 512 * i + 8 * lane, xv[i]);
            ldswv<u2>(raw + 512 * i + 512 + 8 * lane, xv[i + 1]);
            asm volatile("" ::: "memory");
            const u2 t0 = ldstr(ta + 512 * i), t1 = ldstr(ta + 512 * i + 512);
            ldswv<u2>(ua + 2 * ((r0 + 256 * i) & (L - 1)), t0);
            ldswv<u2>(ua + 2 * ((r0 + 256 * i + 256) & (L - 1)), t1);
        }
        ldswv<unsigned char>(sb + FLG + 64 + k, 1);
        STAMP_AT(3);
        const char* gp = p.ws + WS_G + 8 * (64 * k + lane);
        const u64 want = tag << 16;
        u64 gr[8];
#pragma unroll 1
        for (unsigned spin = 0; spin < SPIN_CAP; ++spin) {
            u64 df = 0;
#pragma unroll
            for (int T = 0; T < 8; ++T) gr[T] = ld_agent(gp + 2048 * T);
#pragma unroll
            for (int T = 0; T < 8; ++T) df |= gr[T] ^ want;
            if (__builtin_amdgcn_ballot_w64(((unsigned)(df >> 32) | ((unsigned)df >> 16)) != 0) == 0) break;
            __builtin_amdgcn_s_sleep(4);
        }
        unsigned gw[8];
#pragma unroll
        for (int T = 0; T < 8; ++T) { const float g = bf2f((unsigned)gr[T]); gw[T] = pkbf(g, 1.f - g); }
        STAMP_AT(4);
        const int rr = (lane >> 2) & 3;
        const unsigned oa = sb + ((rr >> 1) + 4 * (rr & 1)) * TB_W + 16 * rr + 8 * (4 * k + (lane >> 4) + 24 * (lane & 3));
        const auto ro = __builtin_amdgcn_make_buffer_rsrc((void*)p.out, 0, L * D * 2, 0x00020000);
        const int ov = (64 * k + lane) * (D * 2) + grp * 8;
#pragma unroll
        for (int T = 0; T < 8; ++T) {
#pragma unroll 1
            for (unsigned spin = 0; spin < SPIN_CAP; ++spin) {
                const u2 fl = ldsrv<u2>(sb + FLG + 8 * T);
                if (__builtin_amdgcn_readfirstlane(fl.x & fl.y) == 0x01010101u) break;
                __builtin_amdgcn_s_sleep(1);
            }
            asm volatile("" ::: "memory");
            const u2 a = ldstr(oa + T * OS_T), b = ldstr(oa + T * OS_T + 2 * TB_W);
            const float o0 = dot2(a.x, gw[T], 0.f), o1 = dot2(a.y, gw[T], 0.f), o2 = dot2(b.x, gw[T], 0.f), o3 = dot2(b.y, gw[T], 0.f);
            __builtin_amdgcn_raw_buffer_store_b64(u2{pkbf(o0, o1), pkbf(o2, o3)}, ro, ov, T * 256 * D * 2, 17);
            STAMP_AT(5 + T);
        }
        asm volatile("s_waitcnt vmcnt(0)" ::: "memory");
        STAMP_AT(15);
        STAMP_OUT();
        return;
    }

    const int c = wv & 3, s = wv >> 2, ch = grp * 4 + c;
    u3 d0, d1, d2;
    {
        const bool up = lane >= 32;
        const u16 *p0 = s ? p.lA : p.Are, *p1 = s ? p.Aimag : p.Aim, *p2 = s ? p.Cr : p.Cre, *p3 = s ? p.Ci : p.Cim;
        const u16 *p4 = s ? p.Cr : p.Bre, *p5 = s ? p.Ci : p.Bim;
        const int o = ch * NM + 6 * (lane & 31);
        d0 = *(const u3*)((up ? p1 : p0) + o);
        d1 = *(const u3*)((up ? p3 : p2) + o);
        d2 = *(const u3*)((up ? p5 : p4) + o);
    }
    const u64 ldt4 = ((const CST4 u64*)(s ? p.Bldt : p.Aldt))[grp], dsk4 = ((const CST4 u64*)(s ? p.BD : p.AD))[grp];
    PIN();
    asm volatile("s_barrier" ::: "memory");

    const unsigned tb = sb + wv * TB_W, ka = sb + KA + wv * KA_W, ub = sb + UO + c * U_C;
    {
        const u4 z4 = {0, 0, 0, 0};
#pragma unroll
        for (int i = 0; i < 5; ++i) ldsw<u4>(ka + 1024 * i + 16 * lane, z4);
        ldsw<u4>(ka + 4736 + 16 * lane, z4);
    }
    asm volatile("s_mov_b32 m0, %0\n\ts_nop 0" ::"s"(tb) : "memory");
    { h2 one = {(_Float16)1.f, (_Float16)0.f}; putrow<0>(one); }
    unsigned wra;
    {
        const int r = lane & 31;
        constexpr u64 t0 = 2ull | 3ull << 5 | 4ull << 10 | 5ull << 15 | 12ull << 20 | 12ull << 25 | 13ull << 30 | 1ull << 35 | 14ull << 40 | 15ull << 45 | 16ull << 50 | 18ull << 55;
        constexpr u64 t1 = 1ull | 1ull << 5 | 6ull << 10 | 7ull << 15 | 19ull << 20 | 20ull << 25 | 21ull << 30 | 22ull << 35 | 8ull << 40 | 9ull << 45 | 10ull << 50 | 11ull << 55;
        constexpr u64 t2 = 2ull | 2ull << 5 | 2ull << 10 | 2ull << 15 | 12ull << 20 | 12ull << 25 | 12ull << 30 | 12ull << 35;
        const u64 tt = r < 12 ? t0 : r < 24 ? t1 : t2;
        const int rr = r < 12 ? r : r < 24 ? r - 12 : r - 24;
        wra = tb + ((int)(tt >> (5 * rr)) & 31) * ROW + 16 * (lane >> 5);
    }
    const unsigned vra = tb + (lane & 31) * ROW + 16 * (lane >> 5);
    const b8 B5 = __builtin_bit_cast(b8, band(lane, 6, 5, 0x3f80u)), B3a = __builtin_bit_cast(b8, band(lane, 3, 3, 0x3f80u));
    const b8 B3b = __builtin_bit_cast(b8, band(lane, 11, 3, 0x3f80u)), B9 = __builtin_bit_cast(b8, band(lane, 4, 9, 0x3f80u));
    const b8 B7 = __builtin_bit_cast(b8, band(lane, 5, 7, 0x3f80u));
    const u4 msp = band(lane, 12, 2, 0xffffu);
    const float dt = rbf(expf(bf2f((unsigned)(ldt4 >> (16 * c)) & 0xffffu)));
    const unsigned dlo = (unsigned)(dsk4 >> (16 * c)) & 0xffffu, dhi = dlo << 16;
    float ka2 = dt * 1.4426950408889634f, kf = dt * 0.15915494309189535f;
    asm volatile("" : "+v"(ka2), "+v"(kf), "+v"(wra));
    PIN();
    STAMP_AT(1);

    float pa[3], pf[3];
    f2 pc[3];
    {
        float cr[3], ci[3];
        spread(d0, pa, pf);
        spread(d1, cr, ci);
#pragma unroll
        for (int j = 0; j < 3; ++j) { pf[j] *= kf; pc[j] = f2{cr[j], ci[j]}; }
        if (s == 0) {
            spread(d2, cr, ci);
#pragma unroll
            for (int j = 0; j < 3; ++j) pc[j] = cmul(pc[j], f2{cr[j], ci[j]});
        } else {
#pragma unroll
            for (int j = 0; j < 3; ++j) pa[j] = -rbf(__builtin_amdgcn_exp2f(pa[j] * 1.4426950408889634f));
        }
    }
    float mx = 0.f;
#pragma unroll
    for (int j = 0; j < 3; ++j) { pa[j] *= ka2; mx = fmaxf(mx, fmaxf(fabsf(pc[j].x), fabsf(pc[j].y))); }
    unsigned mi = __float_as_uint(mx);
    mi = max(mi, (unsigned)__builtin_amdgcn_update_dpp(0, (int)mi, 0x128, 0xf, 0xf, false));
    mi = max(mi, (unsigned)__builtin_amdgcn_update_dpp(0, (int)mi, 0x124, 0xf, 0xf, false));
    mi = max(mi, (unsigned)__builtin_amdgcn_update_dpp(0, (int)mi, 0x122, 0xf, 0xf, false));
    mi = max(mi, (unsigned)__builtin_amdgcn_update_dpp(0, (int)mi, 0x121, 0xf, 0xf, false));
    const unsigned ms = max(max((unsigned)__builtin_amdgcn_readlane(mi, 0), (unsigned)__builtin_amdgcn_readlane(mi, 16)),
                            max((unsigned)__builtin_amdgcn_readlane(mi, 32), (unsigned)__builtin_amdgcn_readlane(mi, 48)));
    const unsigned eb = min(max(ms >> 23, 20u), 240u);
    const float scale = __uint_as_float((267u - eb) << 23);
    const float inv = (s == 0 ? dt : 2.f * dt) * __uint_as_float((eb - 13u) << 23);
    STAMP_AT(2);

    f16v kacc = {};
#pragma unroll 1
    for (int j = 0; j < 3; ++j) {
        const float a = pa[0], f = pf[0];
        const f2 cs = pc[0] * scale;
        pa[0] = pa[1]; pa[1] = pa[2]; pf[0] = pf[1]; pf[1] = pf[2]; pc[0] = pc[1]; pc[1] = pc[2];
        const float t8 = __builtin_amdgcn_fractf(f * 8.f), e8 = __builtin_amdgcn_exp2f(a * 8.f);
        const f2 v1 = f2{__builtin_amdgcn_cosf(t8), -__builtin_amdgcn_sinf(t8)} * e8;
        const float t1 = __builtin_amdgcn_fractf(f), e1 = __builtin_amdgcn_exp2f(a);
        const f2 z1 = f2{__builtin_amdgcn_cosf(t1), __builtin_amdgcn_sinf(t1)} * e1;
        const f2 v2 = cmul(v1, v1), v3 = cmul(v2, v1), v4 = cmul(v2, v2), v8 = cmul(v4, v4), v12 = cmul(v8, v4), v16 = cmul(v8, v8);
        const f2 v20 = cmul(v16, v4), v24 = cmul(v16, v8), v28 = cmul(v16, v12);
        const f2 y256 = cmul(v16, v16), y512 = cmul(y256, y256), y1024 = cmul(y512, y512);
        const h2 P1 = toh(v1), P2 = toh(v2), P3 = toh(v3);
        putrow<1>(P1); putrow<2>(P2); putrow<3>(P3);
#define VROW(B, V) { const h2 Q = toh(V); putrow<B>(Q); putrow<B + 1>(cmulh(Q, P1)); putrow<B + 2>(cmulh(Q, P2)); putrow<B + 3>(cmulh(Q, P3)); }
        VROW(4, v4) VROW(8, v8) VROW(12, v12) VROW(16, v16) VROW(20, v20) VROW(24, v24) VROW(28, v28)
#undef VROW
        h8 vf[8];
#pragma unroll
        for (int q = 0; q < 8; ++q) vf[q] = ldsr<h8>(vra + 32 * q);
        const f2 w1 = cmul(cs, z1), w2 = cmul(w1, z1), w3 = cmul(w2, z1), w4 = cmul(w3, z1);
        const f2 w5 = cmul(w4, z1), w6 = cmul(w5, z1), w7 = cmul(w6, z1), w8 = cmul(w7, z1);
        const h2 W0 = toh(cs), W2 = toh(w2), W4 = toh(w4), W6 = toh(w6), W8 = toh(w8);
        const h2 Y256 = toh(y256), Y512 = toh(y512), Y1024 = toh(y1024);
        putrow<2>(W0); putrow<3>(toh(w1)); putrow<4>(W2); putrow<5>(toh(w3));
        putrow<14>(W4); putrow<15>(toh(w5)); putrow<16>(W6); putrow<18>(toh(w7));
        putrow<12>(cmulhc(W0, Y256)); putrow<13>(cmulhc(W2, Y256)); putrow<1>(cmulhc(W4, Y256)); putrow<6>(cmulhc(W6, Y256)); putrow<7>(cmulhc(W8, Y256));
        const h2 r516 = cmulhc(W4, Y512), r520 = cmulhc(W8, Y512), r1032 = cmulhc(W8, Y1024), r1544 = cmulhc(r1032, Y512);
        putrow<20>(r516); putrow<19>(r520); putrow<9>(cmulhc(r516, Y256)); putrow<8>(cmulhc(r520, Y256));
        putrow<21>(r1032); putrow<22>(cmulhc(r1032, Y256)); putrow<10>(r1544); putrow<11>(cmulhc(r1544, Y256));
        h8 wf[8];
#pragma unroll
        for (int q = 0; q < 8; ++q) wf[q] = ldsr<h8>(wra + 32 * q);
        PIN();
#pragma unroll
        for (int q = 0; q < 8; ++q) kacc = __builtin_amdgcn_mfma_f32_32x32x16_f16(wf[q], vf[q], kacc, 0, 0, 0);
        asm volatile("" ::: "memory");
    }
    STAMP_AT(3);

#pragma unroll 1
    for (unsigned spin = 0; spin < SPIN_CAP; ++spin) {
        if (__builtin_amdgcn_readfirstlane(ldsrv<unsigned>(sb + FLG + 64)) == 0x01010101u) break;
        __builtin_amdgcn_s_sleep(1);
    }
    asm volatile("" ::: "memory");
    STAMP_AT(4);
    const int n = lane & 15, h = lane >> 4;
    const unsigned ga = ub + 32 * n + 16 * h;
    b8 A16[6], A24[6];
#pragma unroll
    for (int I = 0; I < 2; ++I) { A24[I] = ldsr<b8>(ga + 64 * (8 * I + 7) - 16); A16[I] = ldsr<b8>(ga + 64 * (8 * I + 7)); }
    PIN();

    b8 Fs;
    {
        float x[12];
#pragma unroll
        for (int i = 0; i < 12; ++i) x[i] = kacc[i] * inv;
        ldsw<unsigned>(ka + T3O + 4 * (95 - lane), pkbf(x[8], x[9]));
        const unsigned o4 = pkbf(x[10], x[11]);
        const unsigned t4a = ka + T4O + 2 * (127 - 64 * (lane >> 5) - (lane & 31));
        ldsw<u16>(t4a, (u16)o4);
        ldsw<u16>(t4a - 64, (u16)(o4 >> 16));
        Fs = __builtin_bit_cast(b8, msp & (unsigned)__builtin_amdgcn_readlane(pkbf(x[8], x[8]), 63));
        const unsigned D0 = pkbf(x[1], x[0]), D1 = pkbf(x[3], x[2]), D2 = pkbf(x[5], x[4]), D3 = pkbf(x[7], x[6]);
        const unsigned E0 = pkbf(x[2], x[1]), E1 = pkbf(x[4], x[3]), E2 = pkbf(x[6], x[5]), KK = pkbf(x[7], x[7]);
        const unsigned D4 = __builtin_amdgcn_update_dpp(KK, D0, 0x130, 0xf, 0xf, false);
        const unsigned E4 = __builtin_amdgcn_update_dpp(KK, E0, 0x130, 0xf, 0xf, false);
        const unsigned E3 = __builtin_amdgcn_alignbit(D3, D4, 16);
        const unsigned xa = ka + 2 * 536 - 16 * lane;
        ldsw<u4>(xa + X0, u4{E3, E2, E1, E0});
        ldsw<u4>(xa + X1, u4{D4, D3, D2, D1});
        ldsw<u4>(xa + X2, u4{E4, E3, E2, E1});
        ldsw<u4>(xa + X3 + 8, u4{D3, D2, D1, D0});
        if (lane == 0 || lane == 63) {
            const bool b = lane == 0;
            const unsigned KH = KK & 0xffff0000u, x0 = D0 >> 16;
            ldsw<u2>(ka + X0 + (b ? 1088 : 56), b ? u2{x0, 0} : u2{0, KK});
            ldsw<u2>(ka + X1 + (b ? 1088 : 56), b ? u2{D0, 0} : u2{0, KH});
            ldsw<u2>(ka + (b ? X2 + 1088 : X3 + 64), b ? u2{E0, x0} : u2{KH, KK});
        }
    }
    asm volatile("" ::: "memory");
    STAMP_AT(5);

    f4 acc[8];
    b8 F[17], G[64];
    const unsigned fa = ka + (n & 3) * 1216 + ((n & 3) == 3 ? 8 : 0) + 2 * (528 - 4 * (n >> 2) + 8 * h) - 1024;
    const f4 z = {0.f, 0.f, 0.f, 0.f};
    {
        b8 K3[3][2], K4[4];
        const unsigned k3a = ka + T3O + 2 * (64 + 252 - 64 * 4 - 4 * n + 16 * h);
        unsigned k4a = ka + T4O + 4 * (127 - 16 * 7 - n + 4 * h);
        asm volatile("" : "+v"(k4a));
#pragma unroll
        for (int d = 0; d < 4; ++d) {
            u4 r;
#pragma unroll
            for (int e = 0; e < 4; ++e) r[e] = ldsr<unsigned>(k4a + 64 * (3 - d) + 4 * e);
            K4[d] = __builtin_bit_cast(b8, r);
        }
#pragma unroll
        for (int d = 0; d < 3; ++d)
#pragma unroll
            for (int e = 0; e < 2; ++e) K3[d][e] = frag64(k3a + 128 * (2 - d) + 16 * e);
        PIN();
        f4 R[2][6];
        auto box = [&](int I) {
            f4* r = R[I & 1];
            if (I < 4) { r[4] = mma(A24[I], B9, z); r[5] = mma(A16[I], B7, z); }
            r[0] = mma(A24[I], B5, z); r[1] = mma(A16[I], B3a, z); r[2] = mma(A16[I], B5, z); r[3] = mma(A16[I], B3b, z);
            if (I < 4) acc[I + 4] = mma(Fs, A16[I], I == 0 ? z : acc[I + 4]);
        };
        acc[2] = z; acc[3] = z; acc[5] = z; acc[6] = z; acc[7] = z;
        box(0);
        PIN();
#pragma unroll
        for (int I = 0; I < 6; ++I) {
            if (I < 5) box(I + 1);
            if (I < 4) { A24[I + 2] = ldsr<b8>(ga + 64 * (8 * I + 23) - 16); A16[I + 2] = ldsr<b8>(ga + 64 * (8 * I + 23)); }
            if (I == 4) {
#pragma unroll
                for (int t = 7; t >= 0; --t) F[t] = frag64(fa + 64 * (16 - t));
            }
            if (I == 5) {
#pragma unroll
                for (int w = 0; w < 8; ++w) G[w] = ldsr<b8>(ga + 64 * w);
            }
            PIN();
            const f4* r = R[I & 1];
            if (I < 4) {
                const u4 zz = {pkbf(r[4][0], r[5][0]), pkbf(r[4][1], r[5][1]), pkbf(r[4][2], r[5][2]), pkbf(r[4][3], r[5][3])};
#pragma unroll
                for (int d = 0; d < 4; ++d)
                    if (I + 4 + d < 8) acc[I + 4 + d] = mma(__builtin_bit_cast(b8, zz), K4[d], acc[I + 4 + d]);
            }
#pragma unroll
            for (int e = 0; e < 2; ++e) {
                const u4 zz = {pkbf(r[0][2 * e], r[1][2 * e]), pkbf(r[2][2 * e], r[3][2 * e]), pkbf(r[0][2 * e + 1], r[1][2 * e + 1]), pkbf(r[2][2 * e + 1], r[3][2 * e + 1])};
#pragma unroll
                for (int d = 0; d < 3; ++d)
                    if (I + 2 + d < 8) acc[I + 2 + d] = mma(__builtin_bit_cast(b8, zz), K3[d][e], acc[I + 2 + d]);
            }
            PIN();
        }
    }
    STAMP_AT(6);

    const unsigned ea = ub + 2 * (240 + 16 * n + 4 * h);
    const unsigned oa = tb + 16 * (2 * (c & 1) + s) + 8 * (n + 24 * h);
    const unsigned fl = sb + FLG + wv;
    u2 uu;
    auto finish = [&](int T) {
        const float p0 = dot2(uu.x, dlo, acc[T][0]), p1 = dot2(uu.x, dhi, acc[T][1]), p2 = dot2(uu.y, dlo, acc[T][2]), p3 = dot2(uu.y, dhi, acc[T][3]);
        ldswv<u2>(oa + T * OS_T, u2{pkbf(p0, p1), pkbf(p2, p3)});
        ldswv<unsigned char>(fl + 8 * T, 1);
    };
    acc[0] = z; acc[1] = z;
#pragma unroll
    for (int T = 0; T < 8; ++T) {
        const int t0 = T == 0 ? 7 : T == 1 ? 15 : 16;
#pragma unroll
        for (int t = t0; t >= 0; --t) {
            const int k = t0 - t;
            acc[T] = mma(F[t], G[8 * T - t + 7], acc[T]);
            if (T == 0) { F[15 - k] = frag64(fa + 64 * (1 + k)); if (k == 7) F[16] = frag64(fa); }
            if (T >= 1 && k < 8) { const int w = 8 * T + k; G[w] = ldsr<b8>(ga + 64 * w); }
            if (T >= 1 && k == 3) finish(T - 1);
            if (k == t0 - 2) uu = ldsrv<u2>(ea + 512 * T);
            PIN();
        }
        if (T == 0) STAMP_AT(7);
        if (T == 1) STAMP_AT(8);
        if (T == 3) STAMP_AT(9);
        if (T == 5) STAMP_AT(10);
    }
    STAMP_AT(11);
    finish(7);
    STAMP_AT(15);
    STAMP_OUT();
}

extern "C" void kernel_launch(void* const* d_in, const int* in_sizes, int n_in, void* d_out, int out_size, void* d_ws, size_t ws_size,
                              hipStream_t stream) {
    Params p;
    p.x = (const u16*)d_in[0];
    p.Are = (const u16*)d_in[1]; p.Aim = (const u16*)d_in[2]; p.Bre = (const u16*)d_in[3]; p.Bim = (const u16*)d_in[4];
    p.Cre = (const u16*)d_in[5]; p.Cim = (const u16*)d_in[6]; p.Aldt = (const u16*)d_in[7]; p.AD = (const u16*)d_in[8];
    p.lA = (const u16*)d_in[10]; p.Aimag = (const u16*)d_in[11]; p.Cr = (const u16*)d_in[12]; p.Ci = (const u16*)d_in[13];
    p.Bldt = (const u16*)d_in[14]; p.BD = (const u16*)d_in[15]; p.gw = (const u16*)d_in[16];
    p.out = (u16*)d_out;
    p.ws = (char*)d_ws;
    ssm_mix_kernel<<<NWG, NT, 0, stream>>>(p);
}
```

```cpp
#define STAMP 1
#include <hip/hip_runtime.h>
#include <stdint.h>

typedef unsigned short u16;
typedef unsigned long long u64;
typedef float f2 __attribute__((ext_vector_type(2)));
typedef float f4 __attribute__((ext_vector_type(4)));
typedef float f16v __attribute__((ext_vector_type(16)));
typedef _Float16 h2 __attribute__((ext_vector_type(2)));
typedef _Float16 h8 __attribute__((ext_vector_type(8)));
typedef __bf16 b2 __attribute__((ext_vector_type(2)));
typedef __bf16 b8 __attribute__((ext_vector_type(8)));
typedef unsigned u2 __attribute__((ext_vector_type(2)));
typedef unsigned u3 __attribute__((ext_vector_type(3), aligned(4)));
typedef unsigned u4 __attribute__((ext_vector_type(4)));
typedef short s4 __attribute__((ext_vector_type(4)));
#define LDS3 __attribute__((address_space(3)))
#define GLB1 __attribute__((address_space(1)))
#define CST4 __attribute__((address_space(4)))
#define DEV __device__ __forceinline__
#define PIN() __builtin_amdgcn_sched_barrier(0)

constexpr int L = 2048, D = 640, NM = 192;
constexpr int NMAIN = 160, NGATE = 64, NWG = NMAIN + NGATE, NT = 768;
constexpr int TB_W = 8704, ROW = 272;
constexpr int KA = 8 * TB_W, KA_W = 5760;
constexpr int X0 = 0, X1 = 1216, X2 = 2432, X3 = 3656, T3O = 4800, T4O = 5312;
constexpr int UO = KA + 8 * KA_W, U_C = 4576;
constexpr int RAW = UO + 4 * U_C;
constexpr int FLG = RAW + 16384;
constexpr int LDS_TOTAL = FLG + 80;
constexpr int OS_T = 768;
constexpr int WS_START = 256, WS_G = 4096;
constexpr unsigned SPIN_CAP = 1u << 17;

struct Params {
    const u16 *x, *Are, *Aim, *Bre, *Bim, *Cre, *Cim, *Aldt, *AD, *lA, *Aimag, *Cr, *Ci, *Bldt, *BD, *gw;
    u16* out;
    char* ws;
};

DEV float bf2f(unsigned v) { return __uint_as_float(v << 16); }
DEV float bfhi(unsigned v) { return __uint_as_float(v & 0xffff0000u); }
DEV unsigned pkbf(float lo, float hi) { f2 v = {lo, hi}; return __builtin_bit_cast(unsigned, __builtin_convertvector(v, b2)); }
DEV float rbf(float v) { return bfhi(pkbf(v, v)); }
DEV h2 toh(f2 v) { return __builtin_convertvector(v, h2); }
DEV f2 cmul(f2 x, f2 z) {
    f2 t = x * z.xx, r;
    asm("v_pk_fma_f32 %0, %1, %2, %3 op_sel:[1,1,0] op_sel_hi:[0,1,1] neg_lo:[1,0,0]" : "=v"(r) : "v"(x), "v"(z), "v"(t));
    return r;
}
DEV h2 cmulh(h2 x, h2 z) { h2 t = x * z.xx; h2 xs = {-x.y, x.x}; return __builtin_elementwise_fma(xs, z.yy, t); }
DEV h2 cmulhc(h2 x, h2 z) { h2 t = x * z.xx; h2 xs = {x.y, -x.x}; return __builtin_elementwise_fma(xs, z.yy, t); }
template <int R> DEV void putrow(h2 v) { asm volatile("ds_write_addtid_b32 %0 offset:%1" ::"v"(v), "i"(R * ROW) : "memory"); }
template <typename T> DEV T ldsr(unsigned a) { return *(LDS3 T*)a; }
template <typename T> DEV T ldsrv(unsigned a) { return *(volatile LDS3 T*)a; }
template <typename T> DEV void ldsw(unsigned a, T v) { *(LDS3 T*)a = v; }
template <typename T> DEV void ldswv(unsigned a, T v) { *(volatile LDS3 T*)a = v; }
DEV b8 frag64(unsigned a) { u2 p = ldsrv<u2>(a), q = ldsrv<u2>(a + 8); u4 r = {p.x, p.y, q.x, q.y}; return __builtin_bit_cast(b8, r); }
DEV u2 ldstr(unsigned a) { return __builtin_bit_cast(u2, __builtin_amdgcn_ds_read_tr16_b64_v4i16((LDS3 s4*)a)); }
DEV f4 mma(b8 a, b8 b, f4 c) { return __builtin_amdgcn_mfma_f32_16x16x32_bf16(a, b, c, 0, 0, 0); }
DEV float dot2(unsigned a, unsigned b, float c) { return __builtin_amdgcn_fdot2_f32_bf16(__builtin_bit_cast(b2, a), __builtin_bit_cast(b2, b), c, false); }
DEV u64 ld_agent(const char* p) { return __hip_atomic_load((GLB1 u64*)p, __ATOMIC_RELAXED, __HIP_MEMORY_SCOPE_AGENT); }
DEV void st_agent(char* p, u64 v) { __hip_atomic_store((GLB1 u64*)p, v, __ATOMIC_RELAXED, __HIP_MEMORY_SCOPE_AGENT); }
DEV u64 read_tag(const char* ws) {
    u64 e = *(const CST4 u64*)ws;
    asm volatile("" : "+s"(e));
    return e + 1;
}
DEV u4 band(int lane, int first, int width, unsigned val) {
    const unsigned m = ((((1u << width) - 1u) << (first + (lane & 15))) >> (8 * (lane >> 4))) & 0xffu;
    const unsigned t = m | (m << 15);
    u4 r;
#pragma unroll
    for (int i = 0; i < 4; ++i) r[i] = __umul24((t >> (2 * i)) & 0x10001u, val);
    asm volatile("" : "+v"(r));
    return r;
}
DEV void spread(u3 d, float (&P)[3], float (&Q)[3]) {
    const unsigned m[6] = {d.x << 16, d.x & 0xffff0000u, d.y << 16, d.y & 0xffff0000u, d.z << 16, d.z & 0xffff0000u};
#pragma unroll
    for (int j = 0; j < 3; ++j) {
        const auto r = __builtin_amdgcn_permlane32_swap(m[j], m[j + 3], false, false);
        P[j] = __uint_as_float(r[0]);
        Q[j] = __uint_as_float(r[1]);
    }
}
#define STAMP_AT(i) do { if ((i) == 99) { PIN(); dg_t = __builtin_amdgcn_s_memrealtime(); asm volatile("s_waitcnt lgkmcnt(0)" : "+s"(dg_t) :: "memory"); PIN(); } } while (0)
#define STAMP_OUT() do { if (wv == 8 && blk < NMAIN) { \
        for (int q_ = 0; q_ < 2000000; ++q_) { if (__builtin_amdgcn_s_memrealtime() >= rt0 + 1800) break; __builtin_amdgcn_s_sleep(1); } \
        long long mn = 0, mxx = 0; for (int q_ = 0; q_ < 3; ++q_) { int e_ = min(lane + 64 * q_, NMAIN - 1); long long d_ = (long long)(ld_agent(p.ws + 131072 + 8 * e_) - rt0); if (d_ > -1500 && d_ < 1500) { mn = min(mn, d_); mxx = max(mxx, d_); } } \
        for (int o_ = 32; o_ > 0; o_ >>= 1) { mn = min(mn, __shfl_xor(mn, o_)); mxx = max(mxx, __shfl_xor(mxx, o_)); } \
        const u64 lim = rt0 + mn + 3000 + 0; for (int q_ = 0; q_ < 2000000; ++q_) { if (__builtin_amdgcn_s_memrealtime() >= lim) break; __builtin_amdgcn_s_sleep(1); } } } while (0)

DEV void gate_block(const Params& p, int gb) {
    const int tid = threadIdx.x, lane = tid & 63;
    const int row = gb * 32 + (tid >> 4), j = tid & 15;
    const u4* xr = (const u4*)(p.x + row * D);
    const u4* wr = (const u4*)p.gw;
    u4 xv[5], wv[5];
#pragma unroll
    for (int i = 0; i < 5; ++i) { xv[i] = xr[j + 16 * i]; wv[i] = wr[j + 16 * i]; }
    PIN();
    const u64 tag = read_tag(p.ws);
    if (tid == 0) st_agent(p.ws + WS_START + 8 * (NMAIN + gb), tag);
    float s = 0.f;
#pragma unroll
    for (int i = 0; i < 5; ++i)
#pragma unroll
        for (int e = 0; e < 4; ++e) {
            const unsigned q = pkbf(bf2f(xv[i][e]) * bf2f(wv[i][e]), bfhi(xv[i][e]) * bfhi(wv[i][e]));
            s += bf2f(q);
            s += bfhi(q);
        }
#pragma unroll
    for (int o = 8; o > 0; o >>= 1) s += __shfl_xor(s, o);
    const float z = rbf(s);
    const float g = 1.f / (1.f + expf(-z));
    if (j == 0) st_agent(p.ws + WS_G + 8 * row, (tag << 16) | (pkbf(g, g) & 0xffffu));
    if (gb == NGATE - 1 && tid < 64) {
#pragma unroll 1
        for (unsigned spin = 0; spin < SPIN_CAP; ++spin) {
            bool ok = true;
#pragma unroll
            for (int i = 0; i < 4; ++i) {
                const int e = min(lane + 64 * i, NWG - 1);
                ok &= ld_agent(p.ws + WS_START + 8 * e) == tag;
            }
            if (__all(ok)) break;
            __builtin_amdgcn_s_sleep(8);
        }
        if (lane == 0) st_agent(p.ws, tag);
    }
}

__global__ void __launch_bounds__(NT, 3) ssm_mix_kernel(Params p) {
    __shared__ __attribute__((aligned(16))) char smem[LDS_TOTAL];
    const int tid = threadIdx.x, lane = tid & 63;
    const int wv = __builtin_amdgcn_readfirstlane(tid >> 6);
    const int blk = blockIdx.x;
    const u64 rt0 = __builtin_amdgcn_s_memrealtime();
    const u64 dg_0 = rt0;
    u64 dg_t = dg_0;
    if (blk >= NMAIN) {
        if (wv < 8) gate_block(p, blk - NMAIN);
        STAMP_AT(1);
        STAMP_OUT();
        return;
    }
    const unsigned sb = (unsigned)(size_t)(LDS3 char*)smem;
    const int i0 = blk >> 3, grp = (blk & 7) * 20 + i0;

    if (wv >= 8) {
        const int k = wv - 8;
        if (k == 0 && lane == 0) st_agent(p.ws + 131072 + 8 * blk, rt0);
        if (k == 0 && lane < 20) ldsw<unsigned>(sb + FLG + 4 * lane, 0u);
        if (lane < 30) ldsw<u4>(sb + UO + k * U_C + 16 * lane, u4{0, 0, 0, 0});
        const auto rx = __builtin_amdgcn_make_buffer_rsrc((void*)p.x, 0, L * D * 2, 0x00020000);
        const int r0 = 64 * k + ((i0 * 102) & ~63);
        asm volatile("s_waitcnt lgkmcnt(0)\n\ts_barrier" ::: "memory");
        STAMP_AT(1);
        u2 xv[8];
#pragma unroll
        for (int i = 0; i < 8; ++i) xv[i] = __builtin_amdgcn_raw_buffer_load_b64(rx, (((r0 + 256 * i) & (L - 1)) + lane) * (D * 2) + grp * 8, 0, 0);
        PIN();
        STAMP_AT(2);
        const u64 tag = read_tag(p.ws);
        if (k == 0 && lane == 0) st_agent(p.ws + WS_START + 8 * blk, tag);
        const unsigned raw = sb + RAW + 4096 * k;
        const unsigned ta = raw + 8 * ((lane & 48) + 4 * (lane & 3) + ((lane >> 2) & 3));
        const unsigned ua = sb + UO + (lane & 3) * U_C + 2 * (240 + (lane & 60));
#pragma unroll
        for (int i = 0; i < 8; i += 2) {
            ldswv<u2>(raw + 512 * i + 8 * lane, xv[i]);
            ldswv<u2>(raw + 512 * i + 512 + 8 * lane, xv[i + 1]);
            asm volatile("" ::: "memory");
            const u2 t0 = ldstr(ta + 512 * i), t1 = ldstr(ta + 512 * i + 512);
            ldswv<u2>(ua + 2 * ((r0 + 256 * i) & (L - 1)), t0);
            ldswv<u2>(ua + 2 * ((r0 + 256 * i + 256) & (L - 1)), t1);
        }
        ldswv<unsigned char>(sb + FLG + 64 + k, 1);
        STAMP_AT(3);
        const char* gp = p.ws + WS_G + 8 * (64 * k + lane);
        const u64 want = tag << 16;
        u64 gr[8];
#pragma unroll 1
        for (unsigned spin = 0; spin < SPIN_CAP; ++spin) {
            u64 df = 0;
#pragma unroll
            for (int T = 0; T < 8; ++T) gr[T] = ld_agent(gp + 2048 * T);
#pragma unroll
            for (int T = 0; T < 8; ++T) df |= gr[T] ^ want;
            if (__builtin_amdgcn_ballot_w64(((unsigned)(df >> 32) | ((unsigned)df >> 16)) != 0) == 0) break;
            __builtin_amdgcn_s_sleep(4);
        }
        unsigned gw[8];
#pragma unroll
        for (int T = 0; T < 8; ++T) { const float g = bf2f((unsigned)gr[T]); gw[T] = pkbf(g, 1.f - g); }
        STAMP_AT(4);
        const int rr = (lane >> 2) & 3;
        const unsigned oa = sb + ((rr >> 1) + 4 * (rr & 1)) * TB_W + 16 * rr + 8 * (4 * k + (lane >> 4) + 24 * (lane & 3));
        const auto ro = __builtin_amdgcn_make_buffer_rsrc((void*)p.out, 0, L * D * 2, 0x00020000);
        const int ov = (64 * k + lane) * (D * 2) + grp * 8;
#pragma unroll
        for (int T = 0; T < 8; ++T) {
#pragma unroll 1
            for (unsigned spin = 0; spin < SPIN_CAP; ++spin) {
                const u2 fl = ldsrv<u2>(sb + FLG + 8 * T);
                if (__builtin_amdgcn_readfirstlane(fl.x & fl.y) == 0x01010101u) break;
                __builtin_amdgcn_s_sleep(1);
            }
            asm volatile("" ::: "memory");
            const u2 a = ldstr(oa + T * OS_T), b = ldstr(oa + T * OS_T + 2 * TB_W);
            const float o0 = dot2(a.x, gw[T], 0.f), o1 = dot2(a.y, gw[T], 0.f), o2 = dot2(b.x, gw[T], 0.f), o3 = dot2(b.y, gw[T], 0.f);
            __builtin_amdgcn_raw_buffer_store_b64(u2{pkbf(o0, o1), pkbf(o2, o3)}, ro, ov, T * 256 * D * 2, 0);
            STAMP_AT(5 + T);
        }
        asm volatile("s_waitcnt vmcnt(0)" ::: "memory");
        STAMP_AT(15);
        STAMP_OUT();
        return;
    }

    const int c = wv & 3, s = wv >> 2, ch = grp * 4 + c;
    u3 d0, d1, d2;
    {
        const bool up = lane >= 32;
        const u16 *p0 = s ? p.lA : p.Are, *p1 = s ? p.Aimag : p.Aim, *p2 = s ? p.Cr : p.Cre, *p3 = s ? p.Ci : p.Cim;
        const u16 *p4 = s ? p.Cr : p.Bre, *p5 = s ? p.Ci : p.Bim;
        const int o = ch * NM + 6 * (lane & 31);
        d0 = *(const u3*)((up ? p1 : p0) + o);
        d1 = *(const u3*)((up ? p3 : p2) + o);
        d2 = *(const u3*)((up ? p5 : p4) + o);
    }
    const u64 ldt4 = ((const CST4 u64*)(s ? p.Bldt : p.Aldt))[grp], dsk4 = ((const CST4 u64*)(s ? p.BD : p.AD))[grp];
    PIN();
    asm volatile("s_barrier" ::: "memory");

    const unsigned tb = sb + wv * TB_W, ka = sb + KA + wv * KA_W, ub = sb + UO + c * U_C;
    {
        const u4 z4 = {0, 0, 0, 0};
#pragma unroll
        for (int i = 0; i < 5; ++i) ldsw<u4>(ka + 1024 * i + 16 * lane, z4);
        ldsw<u4>(ka + 4736 + 16 * lane, z4);
    }
    asm volatile("s_mov_b32 m0, %0\n\ts_nop 0" ::"s"(tb) : "memory");
    { h2 one = {(_Float16)1.f, (_Float16)0.f}; putrow<0>(one); }
    unsigned wra;
    {
        const int r = lane & 31;
        constexpr u64 t0 = 2ull | 3ull << 5 | 4ull << 10 | 5ull << 15 | 12ull << 20 | 12ull << 25 | 13ull << 30 | 1ull << 35 | 14ull << 40 | 15ull << 45 | 16ull << 50 | 18ull << 55;
        constexpr u64 t1 = 1ull | 1ull << 5 | 6ull << 10 | 7ull << 15 | 19ull << 20 | 20ull << 25 | 21ull << 30 | 22ull << 35 | 8ull << 40 | 9ull << 45 | 10ull << 50 | 11ull << 55;
        constexpr u64 t2 = 2ull | 2ull << 5 | 2ull << 10 | 2ull << 15 | 12ull << 20 | 12ull << 25 | 12ull << 30 | 12ull << 35;
        const u64 tt = r < 12 ? t0 : r < 24 ? t1 : t2;
        const int rr = r < 12 ? r : r < 24 ? r - 12 : r - 24;
        wra = tb + ((int)(tt >> (5 * rr)) & 31) * ROW + 16 * (lane >> 5);
    }
    const unsigned vra = tb + (lane & 31) * ROW + 16 * (lane >> 5);
    const b8 B5 = __builtin_bit_cast(b8, band(lane, 6, 5, 0x3f80u)), B3a = __builtin_bit_cast(b8, band(lane, 3, 3, 0x3f80u));
    const b8 B3b = __builtin_bit_cast(b8, band(lane, 11, 3, 0x3f80u)), B9 = __builtin_bit_cast(b8, band(lane, 4, 9, 0x3f80u));
    const b8 B7 = __builtin_bit_cast(b8, band(lane, 5, 7, 0x3f80u));
    const u4 msp = band(lane, 12, 2, 0xffffu);
    const float dt = rbf(expf(bf2f((unsigned)(ldt4 >> (16 * c)) & 0xffffu)));
    const unsigned dlo = (unsigned)(dsk4 >> (16 * c)) & 0xffffu, dhi = dlo << 16;
    float ka2 = dt * 1.4426950408889634f, kf = dt * 0.15915494309189535f;
    asm volatile("" : "+v"(ka2), "+v"(kf), "+v"(wra));
    PIN();
    STAMP_AT(1);

    float pa[3], pf[3];
    f2 pc[3];
    {
        float cr[3], ci[3];
        spread(d0, pa, pf);
        spread(d1, cr, ci);
#pragma unroll
        for (int j = 0; j < 3; ++j) { pf[j] *= kf; pc[j] = f2{cr[j], ci[j]}; }
        if (s == 0) {
            spread(d2, cr, ci);
#pragma unroll
            for (int j = 0; j < 3; ++j) pc[j] = cmul(pc[j], f2{cr[j], ci[j]});
        } else {
#pragma unroll
            for (int j = 0; j < 3; ++j) pa[j] = -rbf(__builtin_amdgcn_exp2f(pa[j] * 1.4426950408889634f));
        }
    }
    float mx = 0.f;
#pragma unroll
    for (int j = 0; j < 3; ++j) { pa[j] *= ka2; mx = fmaxf(mx, fmaxf(fabsf(pc[j].x), fabsf(pc[j].y))); }
    unsigned mi = __float_as_uint(mx);
    mi = max(mi, (unsigned)__builtin_amdgcn_update_dpp(0, (int)mi, 0x128, 0xf, 0xf, false));
    mi = max(mi, (unsigned)__builtin_amdgcn_update_dpp(0, (int)mi, 0x124, 0xf, 0xf, false));
    mi = max(mi, (unsigned)__builtin_amdgcn_update_dpp(0, (int)mi, 0x122, 0xf, 0xf, false));
    mi = max(mi, (unsigned)__builtin_amdgcn_update_dpp(0, (int)mi, 0x121, 0xf, 0xf, false));
    const unsigned ms = max(max((unsigned)__builtin_amdgcn_readlane(mi, 0), (unsigned)__builtin_amdgcn_readlane(mi, 16)),
                            max((unsigned)__builtin_amdgcn_readlane(mi, 32), (unsigned)__builtin_amdgcn_readlane(mi, 48)));
    const unsigned eb = min(max(ms >> 23, 20u), 240u);
    const float scale = __uint_as_float((267u - eb) << 23);
    const float inv = (s == 0 ? dt : 2.f * dt) * __uint_as_float((eb - 13u) << 23);
    STAMP_AT(2);

    f16v kacc = {};
#pragma unroll 1
    for (int j = 0; j < 3; ++j) {
        const float a = pa[0], f = pf[0];
        const f2 cs = pc[0] * scale;
        pa[0] = pa[1]; pa[1] = pa[2]; pf[0] = pf[1]; pf[1] = pf[2]; pc[0] = pc[1]; pc[1] = pc[2];
        const float t8 = __builtin_amdgcn_fractf(f * 8.f), e8 = __builtin_amdgcn_exp2f(a * 8.f);
        const f2 v1 = f2{__builtin_amdgcn_cosf(t8), -__builtin_amdgcn_sinf(t8)} * e8;
        const float t1 = __builtin_amdgcn_fractf(f), e1 = __builtin_amdgcn_exp2f(a);
        const f2 z1 = f2{__builtin_amdgcn_cosf(t1), __builtin_amdgcn_sinf(t1)} * e1;
        const f2 v2 = cmul(v1, v1), v3 = cmul(v2, v1), v4 = cmul(v2, v2), v8 = cmul(v4, v4), v12 = cmul(v8, v4), v16 = cmul(v8, v8);
        const f2 v20 = cmul(v16, v4), v24 = cmul(v16, v8), v28 = cmul(v16, v12);
        const f2 y256 = cmul(v16, v16), y512 = cmul(y256, y256), y1024 = cmul(y512, y512);
        const h2 P1 = toh(v1), P2 = toh(v2), P3 = toh(v3);
        putrow<1>(P1); putrow<2>(P2); putrow<3>(P3);
#define VROW(B, V) { const h2 Q = toh(V); putrow<B>(Q); putrow<B + 1>(cmulh(Q, P1)); putrow<B + 2>(cmulh(Q, P2)); putrow<B + 3>(cmulh(Q, P3)); }
        VROW(4, v4) VROW(8, v8) VROW(12, v12) VROW(16, v16) VROW(20, v20) VROW(24, v24) VROW(28, v28)
#undef VROW
        h8 vf[8];
#pragma unroll
        for (int q = 0; q < 8; ++q) vf[q] = ldsr<h8>(vra + 32 * q);
        const f2 w1 = cmul(cs, z1), w2 = cmul(w1, z1), w3 = cmul(w2, z1), w4 = cmul(w3, z1);
        const f2 w5 = cmul(w4, z1), w6 = cmul(w5, z1), w7 = cmul(w6, z1), w8 = cmul(w7, z1);
        const h2 W0 = toh(cs), W2 = toh(w2), W4 = toh(w4), W6 = toh(w6), W8 = toh(w8);
        const h2 Y256 = toh(y256), Y512 = toh(y512), Y1024 = toh(y1024);
        putrow<2>(W0); putrow<3>(toh(w1)); putrow<4>(W2); putrow<5>(toh(w3));
        putrow<14>(W4); putrow<15>(toh(w5)); putrow<16>(W6); putrow<18>(toh(w7));
        putrow<12>(cmulhc(W0, Y256)); putrow<13>(cmulhc(W2, Y256)); putrow<1>(cmulhc(W4, Y256)); putrow<6>(cmulhc(W6, Y256)); putrow<7>(cmulhc(W8, Y256));
        const h2 r516 = cmulhc(W4, Y512), r520 = cmulhc(W8, Y512), r1032 = cmulhc(W8, Y1024), r1544 = cmulhc(r1032, Y512);
        putrow<20>(r516); putrow<19>(r520); putrow<9>(cmulhc(r516, Y256)); putrow<8>(cmulhc(r520, Y256));
        putrow<21>(r1032); putrow<22>(cmulhc(r1032, Y256)); putrow<10>(r1544); putrow<11>(cmulhc(r1544, Y256));
        h8 wf[8];
#pragma unroll
        for (int q = 0; q < 8; ++q) wf[q] = ldsr<h8>(wra + 32 * q);
        PIN();
#pragma unroll
        for (int q = 0; q < 8; ++q) kacc = __builtin_amdgcn_mfma_f32_32x32x16_f16(wf[q], vf[q], kacc, 0, 0, 0);
        asm volatile("" ::: "memory");
    }
    STAMP_AT(3);

#pragma unroll 1
    for (unsigned spin = 0; spin < SPIN_CAP; ++spin) {
        if (__builtin_amdgcn_readfirstlane(ldsrv<unsigned>(sb + FLG + 64)) == 0x01010101u) break;
        __builtin_amdgcn_s_sleep(1);
    }
    asm volatile("" ::: "memory");
    STAMP_AT(4);
    const int n = lane & 15, h = lane >> 4;
    const unsigned ga = ub + 32 * n + 16 * h;
    b8 A16[6], A24[6];
#pragma unroll
    for (int I = 0; I < 2; ++I) { A24[I] = ldsr<b8>(ga + 64 * (8 * I + 7) - 16); A16[I] = ldsr<b8>(ga + 64 * (8 * I + 7)); }
    PIN();

    b8 Fs;
    {
        float x[12];
#pragma unroll
        for (int i = 0; i < 12; ++i) x[i] = kacc[i] * inv;
        ldsw<unsigned>(ka + T3O + 4 * (95 - lane), pkbf(x[8], x[9]));
        const unsigned o4 = pkbf(x[10], x[11]);
        const unsigned t4a = ka + T4O + 2 * (127 - 64 * (lane >> 5) - (lane & 31));
        ldsw<u16>(t4a, (u16)o4);
        ldsw<u16>(t4a - 64, (u16)(o4 >> 16));
        Fs = __builtin_bit_cast(b8, msp & (unsigned)__builtin_amdgcn_readlane(pkbf(x[8], x[8]), 63));
        const unsigned D0 = pkbf(x[1], x[0]), D1 = pkbf(x[3], x[2]), D2 = pkbf(x[5], x[4]), D3 = pkbf(x[7], x[6]);
        const unsigned E0 = pkbf(x[2], x[1]), E1 = pkbf(x[4], x[3]), E2 = pkbf(x[6], x[5]), KK = pkbf(x[7], x[7]);
        const unsigned D4 = __builtin_amdgcn_update_dpp(KK, D0, 0x130, 0xf, 0xf, false);
        const unsigned E4 = __builtin_amdgcn_update_dpp(KK, E0, 0x130, 0xf, 0xf, false);
        const unsigned E3 = __builtin_amdgcn_alignbit(D3, D4, 16);
        const unsigned xa = ka + 2 * 536 - 16 * lane;
        ldsw<u4>(xa + X0, u4{E3, E2, E1, E0});
        ldsw<u4>(xa + X1, u4{D4, D3, D2, D1});
        ldsw<u4>(xa + X2, u4{E4, E3, E2, E1});
        ldsw<u4>(xa + X3 + 8, u4{D3, D2, D1, D0});
        if (lane == 0 || lane == 63) {
            const bool b = lane == 0;
            const unsigned KH = KK & 0xffff0000u, x0 = D0 >> 16;
            ldsw<u2>(ka + X0 + (b ? 1088 : 56), b ? u2{x0, 0} : u2{0, KK});
            ldsw<u2>(ka + X1 + (b ? 1088 : 56), b ? u2{D0, 0} : u2{0, KH});
            ldsw<u2>(ka + (b ? X2 + 1088 : X3 + 64), b ? u2{E0, x0} : u2{KH, KK});
        }
    }
    asm volatile("" ::: "memory");
    STAMP_AT(5);

    f4 acc[8];
    b8 F[17], G[64];
    const unsigned fa = ka + (n & 3) * 1216 + ((n & 3) == 3 ? 8 : 0) + 2 * (528 - 4 * (n >> 2) + 8 * h) - 1024;
    const f4 z = {0.f, 0.f, 0.f, 0.f};
    {
        b8 K3[3][2], K4[4];
        const unsigned k3a = ka + T3O + 2 * (64 + 252 - 64 * 4 - 4 * n + 16 * h);
        unsigned k4a = ka + T4O + 4 * (127 - 16 * 7 - n + 4 * h);
        asm volatile("" : "+v"(k4a));
#pragma unroll
        for (int d = 0; d < 4; ++d) {
            u4 r;
#pragma unroll
            for (int e = 0; e < 4; ++e) r[e] = ldsr<unsigned>(k4a + 64 * (3 - d) + 4 * e);
            K4[d] = __builtin_bit_cast(b8, r);
        }
#pragma unroll
        for (int d = 0; d < 3; ++d)
#pragma unroll
            for (int e = 0; e < 2; ++e) K3[d][e] = frag64(k3a + 128 * (2 - d) + 16 * e);
        PIN();
        f4 R[2][6];
        auto box = [&](int I) {
            f4* r = R[I & 1];
            if (I < 4) { r[4] = mma(A24[I], B9, z); r[5] = mma(A16[I], B7, z); }
            r[0] = mma(A24[I], B5, z); r[1] = mma(A16[I], B3a, z); r[2] = mma(A16[I], B5, z); r[3] = mma(A16[I], B3b, z);
            if (I < 4) acc[I + 4] = mma(Fs, A16[I], I == 0 ? z : acc[I + 4]);
        };
        acc[2] = z; acc[3] = z; acc[5] = z; acc[6] = z; acc[7] = z;
        box(0);
        PIN();
#pragma unroll
        for (int I = 0; I < 6; ++I) {
            if (I < 5) box(I + 1);
            if (I < 4) { A24[I + 2] = ldsr<b8>(ga + 64 * (8 * I + 23) - 16); A16[I + 2] = ldsr<b8>(ga + 64 * (8 * I + 23)); }
            if (I == 4) {
#pragma unroll
                for (int t = 7; t >= 0; --t) F[t] = frag64(fa + 64 * (16 - t));
            }
            if (I == 5) {
#pragma unroll
                for (int w = 0; w < 8; ++w) G[w] = ldsr<b8>(ga + 64 * w);
            }
            PIN();
            const f4* r = R[I & 1];
            if (I < 4) {
                const u4 zz = {pkbf(r[4][0], r[5][0]), pkbf(r[4][1], r[5][1]), pkbf(r[4][2], r[5][2]), pkbf(r[4][3], r[5][3])};
#pragma unroll
                for (int d = 0; d < 4; ++d)
                    if (I + 4 + d < 8) acc[I + 4 + d] = mma(__builtin_bit_cast(b8, zz), K4[d], acc[I + 4 + d]);
            }
#pragma unroll
            for (int e = 0; e < 2; ++e) {
                const u4 zz = {pkbf(r[0][2 * e], r[1][2 * e]), pkbf(r[2][2 * e], r[3][2 * e]), pkbf(r[0][2 * e + 1], r[1][2 * e + 1]), pkbf(r[2][2 * e + 1], r[3][2 * e + 1])};
#pragma unroll
                for (int d = 0; d < 3; ++d)
                    if (I + 2 + d < 8) acc[I + 2 + d] = mma(__builtin_bit_cast(b8, zz), K3[d][e], acc[I + 2 + d]);
            }
            PIN();
        }
    }
    STAMP_AT(6);

    const unsigned ea = ub + 2 * (240 + 16 * n + 4 * h);
    const unsigned oa = tb + 16 * (2 * (c & 1) + s) + 8 * (n + 24 * h);
    const unsigned fl = sb + FLG + wv;
    u2 uu;
    auto finish = [&](int T) {
        const float p0 = dot2(uu.x, dlo, acc[T][0]), p1 = dot2(uu.x, dhi, acc[T][1]), p2 = dot2(uu.y, dlo, acc[T][2]), p3 = dot2(uu.y, dhi, acc[T][3]);
        ldswv<u2>(oa + T * OS_T, u2{pkbf(p0, p1), pkbf(p2, p3)});
        ldswv<unsigned char>(fl + 8 * T, 1);
    };
    acc[0] = z; acc[1] = z;
#pragma unroll
    for (int T = 0; T < 8; ++T) {
        const int t0 = T == 0 ? 7 : T == 1 ? 15 : 16;
#pragma unroll
        for (int t = t0; t >= 0; --t) {
            const int k = t0 - t;
            acc[T] = mma(F[t], G[8 * T - t + 7], acc[T]);
            if (T == 0) { F[15 - k] = frag64(fa + 64 * (1 + k)); if (k == 7) F[16] = frag64(fa); }
            if (T >= 1 && k < 8) { const int w = 8 * T + k; G[w] = ldsr<b8>(ga + 64 * w); }
            if (T >= 1 && k == 3) finish(T - 1);
            if (k == t0 - 2) uu = ldsrv<u2>(ea + 512 * T);
            PIN();
        }
        if (T == 0) STAMP_AT(7);
        if (T == 1) STAMP_AT(8);
        if (T == 3) STAMP_AT(9);
        if (T == 5) STAMP_AT(10);
    }
    STAMP_AT(11);
    finish(7);
    STAMP_AT(15);
    STAMP_OUT();
}

extern "C" void kernel_launch(void* const* d_in, const int* in_sizes, int n_in, void* d_out, int out_size, void* d_ws, size_t ws_size,
                              hipStream_t stream) {
    Params p;
    p.x = (const u16*)d_in[0];
    p.Are = (const u16*)d_in[1]; p.Aim = (const u16*)d_in[2]; p.Bre = (const u16*)d_in[3]; p.Bim = (const u16*)d_in[4];
    p.Cre = (const u16*)d_in[5]; p.Cim = (const u16*)d_in[6]; p.Aldt = (const u16*)d_in[7]; p.AD = (const u16*)d_in[8];
    p.lA = (const u16*)d_in[10]; p.Aimag = (const u16*)d_in[11]; p.Cr = (const u16*)d_in[12]; p.Ci = (const u16*)d_in[13];
    p.Bldt = (const u16*)d_in[14]; p.BD = (const u16*)d_in[15]; p.gw = (const u16*)d_in[16];
    p.out = (u16*)d_out;
    p.ws = (char*)d_ws;
    ssm_mix_kernel<<<NWG, NT, 0, stream>>>(p);
}
```

```cpp
#include <hip/hip_runtime.h>
#include <stdint.h>

typedef unsigned short u16;
typedef unsigned long long u64;
typedef float f2 __attribute__((ext_vector_type(2)));
typedef float f4 __attribute__((ext_vector_type(4)));
typedef float f16v __attribute__((ext_vector_type(16)));
typedef _Float16 h2 __attribute__((ext_vector_type(2)));
typedef _Float16 h8 __attribute__((ext_vector_type(8)));
typedef __bf16 b2 __attribute__((ext_vector_type(2)));
typedef __bf16 b8 __attribute__((ext_vector_type(8)));
typedef unsigned u2 __attribute__((ext_vector_type(2)));
typedef unsigned u3 __attribute__((ext_vector_type(3), aligned(4)));
typedef unsigned u4 __attribute__((ext_vector_type(4)));
typedef short s4 __attribute__((ext_vector_type(4)));
#define LDS3 __attribute__((address_space(3)))
#define GLB1 __attribute__((address_space(1)))
#define CST4 __attribute__((address_space(4)))
#define DEV __device__ __forceinline__
#define PIN() __builtin_amdgcn_sched_barrier(0)

constexpr int L = 2048, D = 640, NM = 192;
constexpr int NMAIN = 160, NGATE = 64, NWG = NMAIN + NGATE, NT = 768;
constexpr int TB_W = 6336, ROW = 288;
constexpr int KA = 8 * TB_W, KA_W = 5760;
constexpr int X0 = 0, X1 = 1216, X2 = 2432, X3 = 3656, T3O = 4800, T4O = 5312;
constexpr int UO = KA + 8 * KA_W, U_C = 4576;
constexpr int RAW = UO + 4 * U_C;
constexpr int BND = RAW + 16384;
constexpr int FLG = BND + 6144;
constexpr int LDS_TOTAL = FLG + 80;
constexpr int OS_T = 768;
constexpr int WS_START = 256, WS_G = 4096;
#define ST_AUX 0
constexpr unsigned SPIN_CAP = 1u << 17;

struct Params {
    const u16 *x, *Are, *Aim, *Bre, *Bim, *Cre, *Cim, *Aldt, *AD, *lA, *Aimag, *Cr, *Ci, *Bldt, *BD, *gw;
    u16* out;
    char* ws;
};

DEV float bf2f(unsigned v) { return __uint_as_float(v << 16); }
DEV float bfhi(unsigned v) { return __uint_as_float(v & 0xffff0000u); }
DEV unsigned pkbf(float lo, float hi) { f2 v = {lo, hi}; return __builtin_bit_cast(unsigned, __builtin_convertvector(v, b2)); }
DEV float rbf(float v) { return bfhi(pkbf(v, v)); }
DEV float smul(float a, float b) { return __builtin_fmaf(a, b, 0.f); }
DEV h2 u2h(unsigned v) { return __builtin_bit_cast(h2, v); }
DEV unsigned h2u(h2 v) { return __builtin_bit_cast(unsigned, v); }
DEV h2 toh(f2 v) { return __builtin_convertvector(v, h2); }
DEV f2 cmul(f2 x, f2 z) {
    f2 t = x * z.xx, r;
    asm("v_pk_fma_f32 %0, %1, %2, %3 op_sel:[1,1,0] op_sel_hi:[0,1,1] neg_lo:[1,0,0]" : "=v"(r) : "v"(x), "v"(z), "v"(t));
    return r;
}
DEV f2 sqfin(f2 x, f2 t) {
    f2 r;
    asm("v_pk_fma_f32 %0, %1, %1, %2 op_sel:[1,1,0] op_sel_hi:[0,1,1] neg_lo:[1,0,0]" : "=v"(r) : "v"(x), "v"(t));
    return r;
}
DEV h2 cmulh(h2 x, h2 z) { h2 t = x * z.xx; h2 xs = {-x.y, x.x}; return __builtin_elementwise_fma(xs, z.yy, t); }
DEV h2 cmulhc(h2 x, h2 z) { h2 t = x * z.xx; h2 xs = {x.y, -x.x}; return __builtin_elementwise_fma(xs, z.yy, t); }
template <int R> DEV void putrow(h2 v) { asm volatile("ds_write_addtid_b32 %0 offset:%1" ::"v"(v), "i"(R * ROW) : "memory"); }
template <typename T> DEV T ldsr(unsigned a) { return *(LDS3 T*)a; }
template <typename T> DEV T ldsrv(unsigned a) { return *(volatile LDS3 T*)a; }
template <typename T> DEV void ldsw(unsigned a, T v) { *(LDS3 T*)a = v; }
template <typename T> DEV void ldswv(unsigned a, T v) { *(volatile LDS3 T*)a = v; }
DEV b8 frag64(unsigned a) { u2 p = ldsrv<u2>(a), q = ldsrv<u2>(a + 8); u4 r = {p.x, p.y, q.x, q.y}; return __builtin_bit_cast(b8, r); }
DEV u2 ldstr(unsigned a) { return __builtin_bit_cast(u2, __builtin_amdgcn_ds_read_tr16_b64_v4i16((LDS3 s4*)a)); }
DEV f4 mma(b8 a, b8 b, f4 c) { return __builtin_amdgcn_mfma_f32_16x16x32_bf16(a, b, c, 0, 0, 0); }
DEV float mix(unsigned ab, float g, float gc) {
    float t = bfhi(ab);
    asm("v_mul_f32 %0, %0, %1" : "+v"(t) : "v"(gc));
    asm("v_fmac_f32 %0, %1, %2" : "+v"(t) : "v"(bf2f(ab)), "v"(g));
    return t;
}
DEV u64 ld_agent(const char* p) { return __hip_atomic_load((GLB1 u64*)p, __ATOMIC_RELAXED, __HIP_MEMORY_SCOPE_AGENT); }
DEV void st_agent(char* p, u64 v) { __hip_atomic_store((GLB1 u64*)p, v, __ATOMIC_RELAXED, __HIP_MEMORY_SCOPE_AGENT); }
DEV u64 read_tag(const char* ws) {
    u64 e = *(const CST4 u64*)ws;
    asm volatile("" : "+s"(e));
    return e + 1;
}
DEV u4 band(int lane, int first, int width, unsigned val) {
    const unsigned m = ((((1u << width) - 1u) << (first + (lane & 15))) >> (8 * (lane >> 4))) & 0xffu;
    const unsigned t = m | (m << 15);
    u4 r;
#pragma unroll
    for (int i = 0; i < 4; ++i) r[i] = __umul24((t >> (2 * i)) & 0x10001u, val);
    return r;
}
DEV float exp_bf(float x) {
    const float hi = x * 1.4426950408889634f;
    const float lo = __builtin_fmaf(x, 1.4426950408889634f, -hi) + x * 1.925963033500011e-8f;
    const float e = __builtin_amdgcn_exp2f(hi);
    return rbf(__builtin_fmaf(e, lo * 0.6931471805599453f, e));
}
DEV void spread(u3 d, float (&P)[3], float (&Q)[3]) {
    const unsigned m[6] = {d.x << 16, d.x & 0xffff0000u, d.y << 16, d.y & 0xffff0000u, d.z << 16, d.z & 0xffff0000u};
#pragma unroll
    for (int j = 0; j < 3; ++j) {
        const auto r = __builtin_amdgcn_permlane32_swap(m[j], m[j + 3], false, false);
        P[j] = __uint_as_float(r[0]);
        Q[j] = __uint_as_float(r[1]);
    }
}
#define STAMP_AT(i)
#define STAMP_OUT()

DEV void gate_block(const Params& p, int gb) {
    const int tid = threadIdx.x, lane = tid & 63;
    const int row = gb * 32 + (tid >> 4), j = tid & 15;
    const u4* xr = (const u4*)(p.x + row * D);
    const u4* wr = (const u4*)p.gw;
    u4 xv[5], wv[5];
#pragma unroll
    for (int i = 0; i < 5; ++i) { xv[i] = xr[j + 16 * i]; wv[i] = wr[j + 16 * i]; }
    PIN();
    const u64 tag = read_tag(p.ws);
    if (tid == 0) st_agent(p.ws + WS_START + 8 * (NMAIN + gb), tag);
    float s = 0.f;
#pragma unroll
    for (int i = 0; i < 5; ++i)
#pragma unroll
        for (int e = 0; e < 4; ++e) {
            const unsigned q = pkbf(bf2f(xv[i][e]) * bf2f(wv[i][e]), bfhi(xv[i][e]) * bfhi(wv[i][e]));
            s += bf2f(q);
            s += bfhi(q);
        }
#pragma unroll
    for (int o = 8; o > 0; o >>= 1) s += __shfl_xor(s, o);
    const float z = rbf(s);
    const float g = 1.f / (1.f + expf(-z));
    if (j == 0) st_agent(p.ws + WS_G + 8 * row, (tag << 16) | (pkbf(g, g) & 0xffffu));
    if (gb == NGATE - 1 && tid < 64) {
#pragma unroll 1
        for (unsigned spin = 0; spin < SPIN_CAP; ++spin) {
            bool ok = true;
#pragma unroll
            for (int i = 0; i < 4; ++i) {
                const int e = min(lane + 64 * i, NWG - 1);
                ok &= ld_agent(p.ws + WS_START + 8 * e) == tag;
            }
            if (__all(ok)) break;
            __builtin_amdgcn_s_sleep(8);
        }
        if (lane == 0) st_agent(p.ws, tag);
    }
}

__global__ void __launch_bounds__(NT, 3) ssm_mix_kernel(Params p) {
    __shared__ __attribute__((aligned(16))) char smem[LDS_TOTAL];
    const int tid = threadIdx.x, lane = tid & 63;
    const int wv = __builtin_amdgcn_readfirstlane(tid >> 6);
    const int blk = blockIdx.x;
    if (blk >= NMAIN) {
        if (wv < 8) gate_block(p, blk - NMAIN);
        STAMP_AT(1);
        STAMP_OUT();
        return;
    }
    const unsigned sb = (unsigned)(size_t)(LDS3 char*)smem;
    const int i0 = blk >> 3, grp = (blk & 7) * 20 + i0;

    if (wv >= 8) {
        const int k = wv - 8;
        __builtin_amdgcn_s_setprio(3);
        if (k == 0 && lane < 20) ldsw<unsigned>(sb + FLG + 4 * lane, 0u);
        const auto rx = __builtin_amdgcn_make_buffer_rsrc((void*)p.x, 0, L * D * 2, 0x00020000);
        const int r0 = 64 * k + ((i0 * 102) & ~63);
        asm volatile("s_waitcnt lgkmcnt(0)\n\ts_barrier" ::: "memory");
        STAMP_AT(1);
        u2 xv[8];
#pragma unroll
        for (int i = 0; i < 8; ++i) xv[i] = __builtin_amdgcn_raw_buffer_load_b64(rx, (((r0 + 256 * i) & (L - 1)) + lane) * (D * 2) + grp * 8, 0, 0);
        PIN();
        STAMP_AT(2);
        const u64 tag = read_tag(p.ws);
        if (k == 0 && lane == 0) st_agent(p.ws + WS_START + 8 * blk, tag);
        const unsigned raw = sb + RAW + 4096 * k;
        const unsigned ta = raw + 8 * ((lane & 48) + 4 * (lane & 3) + ((lane >> 2) & 3));
        const unsigned ua = sb + UO + (lane & 3) * U_C + 2 * (240 + (lane & 60));
#pragma unroll
        for (int i = 0; i < 8; i += 2) {
            ldswv<u2>(raw + 512 * i + 8 * lane, xv[i]);
            ldswv<u2>(raw + 512 * i + 512 + 8 * lane, xv[i + 1]);
            asm volatile("" ::: "memory");
            const u2 t0 = ldstr(ta + 512 * i), t1 = ldstr(ta + 512 * i + 512);
            ldswv<u2>(ua + 2 * ((r0 + 256 * i) & (L - 1)), t0);
            ldswv<u2>(ua + 2 * ((r0 + 256 * i + 256) & (L - 1)), t1);
        }
        ldswv<unsigned char>(sb + FLG + 64 + k, 1);
        STAMP_AT(3);
        const char* gp = p.ws + WS_G + 8 * (64 * k + lane);
        const u64 want = tag << 16;
        u64 gr[8];
#pragma unroll 1
        for (unsigned spin = 0; spin < SPIN_CAP; ++spin) {
            u64 df = 0;
#pragma unroll
            for (int T = 0; T < 8; ++T) gr[T] = ld_agent(gp + 2048 * T);
#pragma unroll
            for (int T = 0; T < 8; ++T) df |= gr[T] ^ want;
            if (__builtin_amdgcn_ballot_w64(((unsigned)(df >> 32) | ((unsigned)df >> 16)) != 0) == 0) break;
            __builtin_amdgcn_s_sleep(4);
        }
        float ga_[8], gb_[8];
#pragma unroll
        for (int T = 0; T < 8; ++T) { ga_[T] = bf2f((unsigned)gr[T]); gb_[T] = rbf(1.f - ga_[T]); }
        STAMP_AT(4);
        const int rr = (lane >> 2) & 3;
        const unsigned oa = sb + ((rr >> 1) + 4 * (rr & 1)) * TB_W + 16 * rr + 8 * (4 * k + (lane >> 4) + 24 * (lane & 3));
        const auto ro = __builtin_amdgcn_make_buffer_rsrc((void*)p.out, 0, L * D * 2, 0x00020000);
        const int ov = (64 * k + lane) * (D * 2) + grp * 8;
#pragma unroll
        for (int T = 0; T < 8; ++T) {
#pragma unroll 1
            for (unsigned spin = 0; spin < SPIN_CAP; ++spin) {
                const u2 fl = ldsrv<u2>(sb + FLG + 8 * T);
                if (__builtin_amdgcn_readfirstlane(fl.x & fl.y) == 0x01010101u) break;
                __builtin_amdgcn_s_sleep(1);
            }
            asm volatile("" ::: "memory");
            const u2 a = ldstr(oa + T * OS_T), b = ldstr(oa + T * OS_T + 2 * TB_W);
            const float o0 = mix(a.x, ga_[T], gb_[T]), o1 = mix(a.y, ga_[T], gb_[T]), o2 = mix(b.x, ga_[T], gb_[T]), o3 = mix(b.y, ga_[T], gb_[T]);
            __builtin_amdgcn_raw_buffer_store_b64(u2{pkbf(o0, o1), pkbf(o2, o3)}, ro, ov, T * 256 * D * 2, ST_AUX);
        }
        return;
    }

    const int c = wv & 3, s = wv >> 2, ch = grp * 4 + c;
    __builtin_amdgcn_s_setprio(2);
    if (wv < 6) ldsw<u4>(sb + BND + 1024 * wv + 16 * lane, band(lane, (0xc54b36 >> (4 * wv)) & 15, (0x279335 >> (4 * wv)) & 15, wv == 5 ? 0xffffu : 0x3f80u));
    else if (lane < 60) ldsw<u4>(sb + UO + (2 * (wv - 6) + (lane >= 30)) * U_C + 16 * (lane >= 30 ? lane - 30 : lane), u4{0, 0, 0, 0});
    PIN();
    u3 d0, d1, d2;
    {
        const bool up = lane >= 32;
        const u16 *p0 = s ? p.lA : p.Are, *p1 = s ? p.Aimag : p.Aim, *p2 = s ? p.Cr : p.Cre, *p3 = s ? p.Ci : p.Cim;
        const u16 *p4 = s ? p.Cr : p.Bre, *p5 = s ? p.Ci : p.Bim;
        const int o = ch * NM + 6 * (lane & 31);
        d0 = *(const u3*)((up ? p1 : p0) + o);
        d1 = *(const u3*)((up ? p3 : p2) + o);
        d2 = *(const u3*)((up ? p5 : p4) + o);
    }
    PIN();
    asm volatile("s_waitcnt lgkmcnt(0)\n\ts_barrier" ::: "memory");
    const u64 ldt4 = ((const CST4 u64*)(s ? p.Bldt : p.Aldt))[grp], dsk4 = ((const CST4 u64*)(s ? p.BD : p.AD))[grp];

    const unsigned tb = sb + wv * TB_W, ka = sb + KA + wv * KA_W, ub = sb + UO + c * U_C;
#pragma unroll
    for (int i = 0; i < 5; ++i) ldsw<u4>(ka + 1024 * i + 16 * lane, u4{0, 0, 0, 0});
    ldsw<u4>(ka + 4736 + 16 * lane, u4{0, 0, 0, 0});
    asm volatile("s_mov_b32 m0, %0\n\ts_nop 0" ::"s"(tb) : "memory");
    { h2 one = {(_Float16)1.f, (_Float16)0.f}; putrow<0>(one); }
    const unsigned zra = tb + 16 * (lane >> 4), vra = zra + (lane & 15) * ROW;
    unsigned wra1 = zra + ((unsigned)(0x8765fedcba994321ull >> (4 * (lane & 15))) & 15) * ROW;
    unsigned wra2 = zra + (((unsigned)(0x88883333dc33ba98ull >> (4 * (lane & 15))) & 15) + 8) * ROW;
    const float dt = exp_bf(bf2f((unsigned)(ldt4 >> (16 * c)) & 0xffffu));
    const float dsk = lane == 0 ? bf2f((unsigned)(dsk4 >> (16 * c)) & 0xffffu) : 0.f;
    float ka2 = dt * 1.4426950408889634f, kf = dt * 0.15915494309189535f;
    asm volatile("" : "+v"(ka2), "+v"(kf), "+v"(wra1), "+v"(wra2));
    PIN();
    STAMP_AT(1);

    float pa[3], pf[3];
    f2 pc[3];
    spread(d0, pa, pf);
    if (s != 0) {
#pragma unroll
        for (int j = 0; j < 3; ++j) pa[j] = -exp_bf(pa[j]);
    }
#pragma unroll
    for (int j = 0; j < 3; ++j) { pa[j] *= ka2; pf[j] *= kf; }
    h2 Z[3][11];
    auto chain = [&](int j) {
        const float t1 = __builtin_amdgcn_fractf(pf[j]), e1 = __builtin_amdgcn_exp2f(pa[j]);
        f2 zz = f2{__builtin_amdgcn_cosf(t1), __builtin_amdgcn_sinf(t1)} * e1;
#pragma unroll
        for (int i = 0; i < 11; ++i) { Z[j][i] = toh(zz); if (i < 10) zz = cmul(zz, zz); }
    };
    chain(0);
    {
        float cr[3], ci[3];
        spread(d1, cr, ci);
#pragma unroll
        for (int j = 0; j < 3; ++j) pc[j] = f2{cr[j], ci[j]};
        if (s == 0) {
            spread(d2, cr, ci);
#pragma unroll
            for (int j = 0; j < 3; ++j) pc[j] = cmul(pc[j], f2{cr[j], ci[j]});
        }
    }
    float mx = 0.f;
#pragma unroll
    for (int j = 0; j < 3; ++j) mx = fmaxf(mx, fmaxf(fabsf(pc[j].x), fabsf(pc[j].y)));
    unsigned mi = __float_as_uint(mx);
    mi = max(mi, (unsigned)__builtin_amdgcn_update_dpp(0, (int)mi, 0x128, 0xf, 0xf, false));
    mi = max(mi, (unsigned)__builtin_amdgcn_update_dpp(0, (int)mi, 0x124, 0xf, 0xf, false));
    mi = max(mi, (unsigned)__builtin_amdgcn_update_dpp(0, (int)mi, 0x122, 0xf, 0xf, false));
    mi = max(mi, (unsigned)__builtin_amdgcn_update_dpp(0, (int)mi, 0x121, 0xf, 0xf, false));
    const unsigned ms = max(max((unsigned)__builtin_amdgcn_readlane(mi, 0), (unsigned)__builtin_amdgcn_readlane(mi, 16)),
                            max((unsigned)__builtin_amdgcn_readlane(mi, 32), (unsigned)__builtin_amdgcn_readlane(mi, 48)));
    const unsigned eb = min(max(ms >> 23, 20u), 240u);
    const float scale = __uint_as_float((267u - eb) << 23);
    const float inv = (s == 0 ? dt : 2.f * dt) * __uint_as_float((eb - 13u) << 23);
    h2 C0[3];
#pragma unroll
    for (int j = 0; j < 3; ++j) C0[j] = toh(pc[j] * f2{scale, -scale});
    STAMP_AT(2);

    f4 kacc[4] = {};
    u4 vf[4], zf[4], hf[4];
    h8 wf[2][4];
#define KMM(T, Q) kacc[T] = __builtin_amdgcn_mfma_f32_16x16x32_f16(wf[T & 1][Q], __builtin_bit_cast(h8, T < 2 ? vf[Q] : hf[Q]), kacc[T], 0, 0, 0); PIN();
#define HMM(Q, E) hf[Q][E] = h2u(u2h(vf[Q][E]) * u2h(zf[Q][E]).xx); PIN();
#define HFF(Q, E) { const h2 x_ = u2h(vf[Q][E]); hf[Q][E] = h2u(__builtin_elementwise_fma(h2{-x_.y, x_.x}, u2h(zf[Q][E]).yy, u2h(hf[Q][E]))); } PIN();
#define KM(T, Q) if (j > 0) { KMM(T, Q) }
#define HM(Q, E) if (j > 0) { HMM(Q, E) }
#define HF(Q, E) if (j > 0) { HFF(Q, E) }
#define VM(N, X, Y) V[N] = V[X] * V[Y].xx; PIN();
#define VF(N, X, Y) V[N] = __builtin_elementwise_fma(h2{-V[X].y, V[X].x}, V[Y].yy, V[N]); PIN();
#define CH0 if (j < 2) { t1 = __builtin_amdgcn_fractf(pf[j + 1]); e1 = __builtin_amdgcn_exp2f(pa[j + 1]); } PIN();
#define CH1 if (j < 2) { zz = f2{__builtin_amdgcn_cosf(t1), __builtin_amdgcn_sinf(t1)}; } PIN();
#define CH2 if (j < 2) { zz = zz * e1; } PIN();
#define CHA if (j < 2) { zt = zz * zz.xx; } PIN();
#define CHC(I) if (j < 2) { Z[j + 1][I] = toh(zz); } PIN();
#define CHB if (j < 2) { zz = sqfin(zz, zt); } PIN();
#define PM(N, X, Y) const h2 N##_t = X * Y.xx; PIN();
#define PF(N, X, Y) const h2 N = __builtin_elementwise_fma(h2{X.y, -X.x}, Y.yy, N##_t); PIN();
#define VW(R) putrow<R>(V[R]);
#pragma unroll
    for (int j = 0; j < 3; ++j) {
        if (j == 1) __builtin_amdgcn_s_setprio(1);
        if (j == 2) __builtin_amdgcn_s_setprio(0);
        const h2* z = Z[j];
        h2 V[17];
        f2 zz, zt;
        float t1, e1;
        V[1] = z[3]; V[2] = z[4]; V[4] = z[5]; V[8] = z[6]; V[16] = z[7];
        KM(0, 0) HM(0, 0) HM(0, 1) KM(1, 0) HM(0, 2) HM(0, 3) HF(0, 0) HF(0, 1) HF(0, 2) HF(0, 3) KM(0, 1) HM(1, 0) HM(1, 1) KM(1, 1) HM(1, 2)
        HM(1, 3) KM(2, 0) HF(1, 0) HF(1, 1) KM(3, 0) HF(1, 2) HF(1, 3) KM(0, 2) HM(2, 0) HM(2, 1) KM(1, 2) HM(2, 2) HM(2, 3) KM(2, 1) HF(2, 0)
        HF(2, 1) KM(3, 1) HF(2, 2) HF(2, 3) KM(0, 3) HM(3, 0) HM(3, 1) KM(1, 3) HM(3, 2) HM(3, 3) KM(2, 2) HF(3, 0) HF(3, 1) KM(3, 2) HF(3, 2)
        HF(3, 3)
        VM(3, 2, 1) VM(12, 8, 4) KM(2, 3) VF(3, 2, 1) VM(5, 4, 1) KM(3, 3) VF(12, 8, 4) VM(6, 4, 2) VF(5, 4, 1) VM(7, 4, 3) VF(6, 4, 2) VM(9, 8, 1)
        VF(7, 4, 3) VM(10, 8, 2) VF(9, 8, 1) VM(11, 8, 3) VF(10, 8, 2) VM(13, 12, 1) VF(11, 8, 3) VM(14, 12, 2) VF(13, 12, 1) VM(15, 12, 3)
        VF(14, 12, 2) VF(15, 12, 3)
        const h2 W0 = C0[j];
        CH0 PM(W1, W0, z[0]) PM(W2, W0, z[1]) CH1 PF(W1, W0, z[0]) PM(W4, W0, z[2]) CH2 PF(W2, W0, z[1]) PM(W3, W1, z[1]) CHA CHC(0) PF(W4, W0, z[2])
        PM(W8, W0, z[3]) CHB PF(W3, W1, z[1]) PM(W5, W1, z[2]) CHA CHC(1) PF(W8, W0, z[3]) PM(W6, W2, z[2]) CHB PF(W5, W1, z[2]) PM(W7, W3, z[2]) CHA
        CHC(2) PF(W6, W2, z[2]) PM(r256, W0, z[8]) CHB PF(W7, W3, z[2]) PM(r258, W2, z[8]) CHA CHC(3) PF(r256, W0, z[8]) PM(r260, W4, z[8]) CHB
        PF(r258, W2, z[8]) PM(r262, W6, z[8]) CHA CHC(4) PF(r260, W4, z[8]) PM(r264, W8, z[8]) CHB PF(r262, W6, z[8]) PM(r516, W4, z[9]) CHA CHC(5)
        PF(r264, W8, z[8]) PM(r520, W8, z[9]) CHB PF(r516, W4, z[9]) PM(r1032, W8, z[10]) CHA CHC(6) PF(r520, W8, z[9]) PM(r772, r516, z[8]) CHB
        PF(r1032, W8, z[10]) PM(r1544, r1032, z[9]) CHA CHC(7) PF(r772, r516, z[8]) PM(r776, r520, z[8]) CHB PF(r1544, r1032, z[9])
        PM(r1288, r1032, z[8]) CHA CHC(8) PF(r776, r520, z[8]) PM(r1800, r1544, z[8]) CHB PF(r1288, r1032, z[8]) CHA CHC(9) CHB CHC(10)
        PF(r1800, r1544, z[8])
        VW(1) VW(2) VW(3) VW(4) VW(5) VW(6) VW(7) VW(8) VW(9) VW(10) VW(11) VW(12) VW(13) VW(14) VW(15) VW(16)
#pragma unroll
        for (int q = 0; q < 4; ++q) { vf[q] = ldsr<u4>(vra + 64 * q); zf[q] = ldsr<u4>(zra + 16 * ROW + 64 * q); }
        putrow<1>(W0); putrow<2>(W1); putrow<3>(W2); putrow<4>(W3); putrow<5>(r772); putrow<6>(r776); putrow<7>(r1800); putrow<8>(r1544);
        putrow<9>(r256); putrow<10>(r258); putrow<11>(r260); putrow<12>(r516); putrow<13>(r520); putrow<14>(r1288); putrow<15>(r1032); putrow<16>(W4);
        putrow<17>(W5); putrow<18>(W6); putrow<19>(W7); putrow<20>(r262); putrow<21>(r264);
#pragma unroll
        for (int q = 0; q < 4; ++q) { wf[0][q] = ldsr<h8>(wra1 + 64 * q); wf[1][q] = ldsr<h8>(wra2 + 64 * q); }
        PIN();
    }
    KMM(0, 0) HMM(0, 0) HMM(0, 1) KMM(1, 0) HMM(0, 2) HMM(0, 3) HFF(0, 0) HFF(0, 1) HFF(0, 2) HFF(0, 3) KMM(0, 1) HMM(1, 0) HMM(1, 1) KMM(1, 1)
        HMM(1, 2) HMM(1, 3) KMM(2, 0) HFF(1, 0) HFF(1, 1) KMM(3, 0) HFF(1, 2) HFF(1, 3) KMM(0, 2) HMM(2, 0) HMM(2, 1) KMM(1, 2) HMM(2, 2) HMM(2, 3)
        KMM(2, 1) HFF(2, 0) HFF(2, 1) KMM(3, 1) HFF(2, 2) HFF(2, 3) KMM(0, 3) HMM(3, 0) HMM(3, 1) KMM(1, 3) HMM(3, 2) HMM(3, 3) KMM(2, 2) HFF(3, 0)
        HFF(3, 1) KMM(3, 2) HFF(3, 2) HFF(3, 3) KMM(2, 3) KMM(3, 3)
#undef KMM
#undef HMM
#undef HFF
#undef KM
#undef HM
#undef HF
#undef VM
#undef VF
#undef CH0
#undef CH1
#undef CH2
#undef CHA
#undef CHB
#undef CHC
#undef PM
#undef PF
#undef VW
    STAMP_AT(3);
    __builtin_amdgcn_s_setprio(2);

#pragma unroll 1
    for (unsigned spin = 0; spin < SPIN_CAP; ++spin) {
        if (__builtin_amdgcn_readfirstlane(ldsrv<unsigned>(sb + FLG + 64)) == 0x01010101u) break;
        __builtin_amdgcn_s_sleep(1);
    }
    asm volatile("" ::: "memory");
    STAMP_AT(4);
    const int n = lane & 15, h = lane >> 4;
    const unsigned ga = ub + 32 * n + 16 * h;
    b8 A16[6], A24[6];
#pragma unroll
    for (int I = 0; I < 2; ++I) { A24[I] = ldsr<b8>(ga + 64 * (8 * I + 7) - 16); A16[I] = ldsr<b8>(ga + 64 * (8 * I + 7)); }
    const unsigned ba = sb + BND + 16 * lane;
    const b8 B5 = ldsr<b8>(ba), B3a = ldsr<b8>(ba + 1024), B3b = ldsr<b8>(ba + 2048), B9 = ldsr<b8>(ba + 3072), B7 = ldsr<b8>(ba + 4096);
    const u4 msp = ldsr<u4>(ba + 5120);
    PIN();

    unsigned Dn[4], Df[4];
    auto pack = [&](int i) {
        const f4 lo = kacc[i >> 1], hi = kacc[2 + (i >> 1)];
        const unsigned dl = pkbf(lo[2 * (i & 1) + 1] * inv, __builtin_fmaf(lo[2 * (i & 1)], inv, i == 0 ? dsk : 0.f)), dh = pkbf(smul(hi[2 * (i & 1) + 1], inv), hi[2 * (i & 1)] * inv);
        const auto r = __builtin_amdgcn_permlane16_swap(dl, dh, false, false);
        const auto t = __builtin_amdgcn_permlane32_swap(r[0], r[1], false, false);
        Dn[i] = t[0]; Df[i] = t[1];
    };
    pack(0); pack(1);
    ldswv<unsigned>(ka + T3O + 4 * (95 - lane), Df[0]);
    const unsigned t4a = ka + T4O + 2 * (95 - 64 * (lane >> 5) - (lane & 31));
    ldswv<u16>(t4a + 64, (u16)Df[1]);
    ldswv<u16>(t4a, (u16)(Df[1] >> 16));
    asm volatile("" ::: "memory");
    b8 K3[3][2], K4[4];
    {
        const unsigned k3a = ka + T3O + 2 * (64 + 252 - 64 * 4 - 4 * n + 16 * h);
        unsigned k4a = ka + T4O + 4 * (127 - 16 * 7 - n + 4 * h);
        asm volatile("" : "+v"(k4a));
#pragma unroll
        for (int d = 0; d < 4; ++d) {
            u4 r;
#pragma unroll
            for (int e = 0; e < 4; ++e) r[e] = ldsr<unsigned>(k4a + 64 * (3 - d) + 4 * e);
            K4[d] = __builtin_bit_cast(b8, r);
        }
#pragma unroll
        for (int d = 0; d < 3; ++d)
#pragma unroll
            for (int e = 0; e < 2; ++e) K3[d][e] = frag64(k3a + 128 * (2 - d) + 16 * e);
    }
    const b8 Fs = __builtin_bit_cast(b8, msp & (((unsigned)__builtin_amdgcn_readlane(Df[0], 63) & 0xffffu) * 0x10001u));
    PIN();
    STAMP_AT(5);

    f4 acc[8];
    b8 F[17], G[64];
    const unsigned fa = ka + (n & 3) * 1216 + ((n & 3) == 3 ? 8 : 0) + 2 * (528 - 4 * (n >> 2) + 8 * h) - 1024;
    const f4 z = {0.f, 0.f, 0.f, 0.f};
    {
        f4 R[2][6];
        auto box = [&](int I) {
            f4* r = R[I & 1];
            if (I < 4) { r[4] = mma(A24[I], B9, z); r[5] = mma(A16[I], B7, z); }
            r[0] = mma(A24[I], B5, z); r[1] = mma(A16[I], B3a, z); r[2] = mma(A16[I], B5, z); r[3] = mma(A16[I], B3b, z);
            if (I < 4) acc[I + 4] = mma(Fs, A16[I], I == 0 ? z : acc[I + 4]);
        };
        acc[2] = z; acc[3] = z; acc[5] = z; acc[6] = z; acc[7] = z;
        box(0);
        pack(2); pack(3);
        const unsigned D0 = Dn[0], D1 = Dn[1], D2 = Dn[2], D3 = Dn[3];
        const unsigned E0 = __builtin_amdgcn_alignbit(D0, D1, 16), E1 = __builtin_amdgcn_alignbit(D1, D2, 16), E2 = __builtin_amdgcn_alignbit(D2, D3, 16);
        const unsigned KK = __builtin_amdgcn_perm(D3, D3, 0x01000100u);
        const unsigned D4 = __builtin_amdgcn_update_dpp(KK, D0, 0x130, 0xf, 0xf, false);
        const unsigned E4 = __builtin_amdgcn_update_dpp(KK, E0, 0x130, 0xf, 0xf, false);
        const unsigned E3 = __builtin_amdgcn_alignbit(D3, D4, 16);
        const unsigned xa = ka + 2 * 536 - 16 * lane;
        ldswv<u4>(xa + X2, u4{E4, E3, E2, E1}); ldswv<u4>(xa + X1, u4{D4, D3, D2, D1});
        PIN();
#pragma unroll
        for (int I = 0; I < 6; ++I) {
            if (I < 5) box(I + 1);
            if (I < 4) { A24[I + 2] = ldsr<b8>(ga + 64 * (8 * I + 23) - 16); A16[I + 2] = ldsr<b8>(ga + 64 * (8 * I + 23)); }
            if (I == 0) { ldswv<u4>(xa + X0, u4{E3, E2, E1, E0}); ldswv<u4>(xa + X3 + 8, u4{D3, D2, D1, D0}); }
            if (I == 1 && (lane == 0 || lane == 63)) {
                const bool b = lane == 0;
                const unsigned KH = KK & 0xffff0000u, x0 = D0 >> 16;
                ldswv<u2>(ka + X0 + (b ? 1088 : 56), b ? u2{x0, 0} : u2{0, KK});
                ldswv<u2>(ka + X1 + (b ? 1088 : 56), b ? u2{D0, 0} : u2{0, KH});
                ldswv<u2>(ka + (b ? X2 + 1088 : X3 + 64), b ? u2{E0, x0} : u2{KH, KK});
            }
            if (I == 4) {
#pragma unroll
                for (int t = 7; t >= 0; --t) F[t] = frag64(fa + 64 * (16 - t));
            }
            if (I == 5) {
#pragma unroll
                for (int w = 0; w < 8; ++w) G[w] = ldsr<b8>(ga + 64 * w);
            }
            PIN();
            const f4* r = R[I & 1];
            if (I < 4) {
                const u4 zz = {pkbf(r[4][0], r[5][0]), pkbf(r[4][1], r[5][1]), pkbf(r[4][2], r[5][2]), pkbf(r[4][3], r[5][3])};
#pragma unroll
                for (int d = 0; d < 4; ++d)
                    if (I + 4 + d < 8) acc[I + 4 + d] = mma(__builtin_bit_cast(b8, zz), K4[d], acc[I + 4 + d]);
            }
#pragma unroll
            for (int e = 0; e < 2; ++e) {
                const u4 zz = {pkbf(r[0][2 * e], r[1][2 * e]), pkbf(r[2][2 * e], r[3][2 * e]), pkbf(r[0][2 * e + 1], r[1][2 * e + 1]), pkbf(r[2][2 * e + 1], r[3][2 * e + 1])};
#pragma unroll
                for (int d = 0; d < 3; ++d)
                    if (I + 2 + d < 8) acc[I + 2 + d] = mma(__builtin_bit_cast(b8, zz), K3[d][e], acc[I + 2 + d]);
            }
            PIN();
        }
    }
    STAMP_AT(6);

    const unsigned oa = tb + 16 * (2 * (c & 1) + s) + 8 * (n + 24 * h);
    const unsigned fl = sb + FLG + wv;
    auto finish = [&](int T) {
        ldswv<u2>(oa + T * OS_T, u2{pkbf(acc[T][0], acc[T][1]), pkbf(acc[T][2], acc[T][3])});
        ldswv<unsigned char>(fl + 8 * T, 1);
    };
    acc[0] = z; acc[1] = z;
#pragma unroll
    for (int T = 0; T < 8; ++T) {
        const int t0 = T == 0 ? 7 : T == 1 ? 15 : 16;
        if (T == 0) __builtin_amdgcn_s_setprio(1);
        if (T == 4) __builtin_amdgcn_s_setprio(0);
#pragma unroll
        for (int t = t0; t >= 0; --t) {
            const int k = t0 - t;
            acc[T] = mma(F[t], G[8 * T - t + 7], acc[T]);
            if (T == 0) { F[15 - k] = frag64(fa + 64 * (1 + k)); if (k == 7) F[16] = frag64(fa); }
            if (T >= 1 && k < 8) { const int w = 8 * T + k; G[w] = ldsr<b8>(ga + 64 * w); }
            if (T >= 1 && k == 3) finish(T - 1);
            PIN();
        }
        if (T == 0) STAMP_AT(7);
        if (T == 1) STAMP_AT(8);
        if (T == 3) STAMP_AT(9);
        if (T == 5) STAMP_AT(10);
    }
    STAMP_AT(11);
    finish(7);
    STAMP_AT(15);
    STAMP_OUT();
}

extern "C" void kernel_launch(void* const* d_in, const int* in_sizes, int n_in, void* d_out, int out_size, void* d_ws, size_t ws_size,
                              hipStream_t stream) {
    Params p;
    p.x = (const u16*)d_in[0];
    p.Are = (const u16*)d_in[1]; p.Aim = (const u16*)d_in[2]; p.Bre = (const u16*)d_in[3]; p.Bim = (const u16*)d_in[4];
    p.Cre = (const u16*)d_in[5]; p.Cim = (const u16*)d_in[6]; p.Aldt = (const u16*)d_in[7]; p.AD = (const u16*)d_in[8];
    p.lA = (const u16*)d_in[10]; p.Aimag = (const u16*)d_in[11]; p.Cr = (const u16*)d_in[12]; p.Ci = (const u16*)d_in[13];
    p.Bldt = (const u16*)d_in[14]; p.BD = (const u16*)d_in[15]; p.gw = (const u16*)d_in[16];
    p.out = (u16*)d_out;
    p.ws = (char*)d_ws;
    ssm_mix_kernel<<<NWG, NT, 0, stream>>>(p);
}
```

```cpp
#include <hip/hip_runtime.h>
#include <stdint.h>

typedef unsigned short u16;
typedef unsigned long long u64;
typedef float f2 __attribute__((ext_vector_type(2)));
typedef float f4 __attribute__((ext_vector_type(4)));
typedef _Float16 h2 __attribute__((ext_vector_type(2)));
typedef _Float16 h8 __attribute__((ext_vector_type(8)));
typedef __bf16 b2 __attribute__((ext_vector_type(2)));
typedef __bf16 b8 __attribute__((ext_vector_type(8)));
typedef unsigned u2 __attribute__((ext_vector_type(2)));
typedef unsigned u3 __attribute__((ext_vector_type(3), aligned(4)));
typedef unsigned u4 __attribute__((ext_vector_type(4)));
typedef short s4 __attribute__((ext_vector_type(4)));
#define LDS3 __attribute__((address_space(3)))
#define GLB1 __attribute__((address_space(1)))
#define CST4 __attribute__((address_space(4)))
#define DEV __device__ __forceinline__
#define PIN() __builtin_amdgcn_sched_barrier(0)
#define PRIO(n) __builtin_amdgcn_s_setprio(n)

constexpr int L = 2048, D = 640, NM = 192;
constexpr int NMAIN = 160, NGATE = 64, NWG = NMAIN + NGATE, NT = 768;
constexpr int TB_W = 6336, ROW = 288;
constexpr int KA = 8 * TB_W, KA_W = 5760;
constexpr int X0 = 0, X1 = 1216, X2 = 2432, X3 = 3656, T3O = 4800, T4O = 5312;
constexpr int UO = KA + 8 * KA_W, U_C = 4576;
constexpr int RAW = UO + 4 * U_C;
constexpr int BND = RAW + 16384;
constexpr int FLG = BND + 6144;
constexpr int LDS_TOTAL = FLG + 80;
constexpr int OS_T = 768;
constexpr int WS_START = 256, WS_G = 4096;
constexpr unsigned SPIN_CAP = 1u << 20;

struct Params {
    const u16 *x, *gw;
    u16* out;
    char* ws;
    const u16* q[2][8];
};
struct Head { const u16 *x, *gw; u16* out; char* ws; };
struct Ssm { u64 q[8]; };
DEV const CST4 u64* args() { return (const CST4 u64*)__builtin_amdgcn_kernarg_segment_ptr(); }
DEV Head args_head() { const CST4 u64* a = args(); return Head{(const u16*)a[0], (const u16*)a[1], (u16*)a[2], (char*)a[3]}; }
DEV Ssm args_ssm(int s) { const CST4 u64* a = args() + 4 + 8 * s; return Ssm{{a[0], a[1], a[2], a[3], a[4], a[5], a[6], a[7]}}; }

DEV float bf2f(unsigned v) { return __uint_as_float(v << 16); }
DEV float bfhi(unsigned v) { return __uint_as_float(v & 0xffff0000u); }
DEV unsigned pkbf(float lo, float hi) { f2 v = {lo, hi}; return __builtin_bit_cast(unsigned, __builtin_convertvector(v, b2)); }
DEV float rbf(float v) { return bfhi(pkbf(v, v)); }
DEV float smul(float a, float b) { return __builtin_fmaf(a, b, 0.f); }
DEV b8 b8c(u4 v) { return __builtin_bit_cast(b8, v); }
DEV h2 u2h(unsigned v) { return __builtin_bit_cast(h2, v); }
DEV unsigned h2u(h2 v) { return __builtin_bit_cast(unsigned, v); }
DEV h2 toh(f2 v) { return __builtin_convertvector(v, h2); }
DEV f2 cfin(f2 x, f2 z, f2 t) {
    f2 r;
    asm("v_pk_fma_f32 %0, %1, %2, %3 op_sel:[1,1,0] op_sel_hi:[0,1,1] neg_lo:[1,0,0]" : "=v"(r) : "v"(x), "v"(z), "v"(t));
    return r;
}
template <int R> DEV void putrow(h2 v) { asm volatile("ds_write_addtid_b32 %0 offset:%1" ::"v"(v), "i"(R * ROW) : "memory"); }
template <typename T> DEV T ldsr(unsigned a) { return *(LDS3 T*)a; }
template <typename T> DEV T ldsrv(unsigned a) { return *(volatile LDS3 T*)a; }
template <typename T> DEV void ldsw(unsigned a, T v) { *(LDS3 T*)a = v; }
template <typename T> DEV void ldswv(unsigned a, T v) { *(volatile LDS3 T*)a = v; }
DEV b8 frag64(unsigned a) { const u2 p = ldsrv<u2>(a), q = ldsrv<u2>(a + 8); return b8c(u4{p.x, p.y, q.x, q.y}); }
DEV b8 frag32(unsigned a) { return b8c(u4{ldsr<unsigned>(a), ldsr<unsigned>(a + 4), ldsr<unsigned>(a + 8), ldsr<unsigned>(a + 12)}); }
DEV u2 ldstr(unsigned a) { return __builtin_bit_cast(u2, __builtin_amdgcn_ds_read_tr16_b64_v4i16((LDS3 s4*)a)); }
DEV f4 mma(b8 a, b8 b, f4 c) { return __builtin_amdgcn_mfma_f32_16x16x32_bf16(a, b, c, 0, 0, 0); }
DEV float mix(unsigned ab, float g, float gc) {
    float t = bfhi(ab);
    asm("v_mul_f32 %0, %0, %1" : "+v"(t) : "v"(gc));
    asm("v_fmac_f32 %0, %1, %2" : "+v"(t) : "v"(bf2f(ab)), "v"(g));
    return t;
}
DEV unsigned wl(unsigned v, int l, unsigned old) { asm("v_writelane_b32 %0, %1, %2" : "+v"(old) : "s"(v), "i"(l)); return old; }
DEV float max3abs(float a, float b, float c) { float r; asm("v_max3_f32 %0, |%1|, |%2|, |%3|" : "=v"(r) : "v"(a), "v"(b), "v"(c)); return r; }
DEV u64 ld_agent(const char* p) { return __hip_atomic_load((GLB1 u64*)p, __ATOMIC_RELAXED, __HIP_MEMORY_SCOPE_AGENT); }
DEV void st_agent(char* p, u64 v) { __hip_atomic_store((GLB1 u64*)p, v, __ATOMIC_RELAXED, __HIP_MEMORY_SCOPE_AGENT); }
DEV u64 read_tag(const char* ws) { return ld_agent(ws) + 1; }
DEV void wait4(unsigned v, unsigned a) {
    if (__builtin_amdgcn_readfirstlane(v) != 0x01010101u) {
#pragma unroll 1
        for (unsigned spin = 0; spin < SPIN_CAP; ++spin) {
            if (__builtin_amdgcn_readfirstlane(ldsrv<unsigned>(a)) == 0x01010101u) break;
            __builtin_amdgcn_s_sleep(1);
        }
    }
    asm volatile("" ::: "memory");
}
DEV u4 band(int lane, int first, int width, unsigned val) {
    const unsigned m = ((((1u << width) - 1u) << (first + (lane & 15))) >> (8 * (lane >> 4))) & 0xffu;
    const unsigned t = m | (m << 15);
    u4 r;
#pragma unroll
    for (int i = 0; i < 4; ++i) r[i] = __umul24((t >> (2 * i)) & 0x10001u, val);
    return r;
}
DEV float exp_bf(float x) {
    const float hi = x * 1.4426950408889634f;
    const float lo = __builtin_fmaf(x, 1.4426950408889634f, -hi) + x * 1.925963033500011e-8f;
    const float e = __builtin_amdgcn_exp2f(hi);
    return rbf(__builtin_fmaf(e, lo * 0.6931471805599453f, e));
}

DEV void gate_block(int gb) {
    const Head p = args_head();
    const int tid = threadIdx.x, lane = tid & 63;
    const int row = gb * 32 + (tid >> 4), j = tid & 15;
    const GLB1 u4* xr = (const GLB1 u4*)(p.x + row * D);
    const GLB1 u4* wr = (const GLB1 u4*)p.gw;
    const u64 tag = read_tag(p.ws);
    u4 xv[5], wv[5];
#pragma unroll
    for (int i = 0; i < 5; ++i) { xv[i] = xr[j + 16 * i]; wv[i] = wr[j + 16 * i]; }
    PIN();
    float s = 0.f;
#pragma unroll
    for (int i = 0; i < 5; ++i)
#pragma unroll
        for (int e = 0; e < 4; ++e) {
            const unsigned q = pkbf(bf2f(xv[i][e]) * bf2f(wv[i][e]), bfhi(xv[i][e]) * bfhi(wv[i][e]));
            s += bf2f(q);
            s += bfhi(q);
        }
#pragma unroll
    for (int o = 8; o > 0; o >>= 1) s += __shfl_xor(s, o);
    const float g = 1.f / (1.f + expf(-rbf(s)));
    if (j == 0) st_agent(p.ws + WS_G + 8 * row, (tag << 16) | (pkbf(g, g) & 0xffffu));
    if (gb == NGATE - 1 && tid < 64) {
#pragma unroll 1
        for (unsigned spin = 0; spin < SPIN_CAP; ++spin) {
            bool ok = true;
#pragma unroll
            for (int i = 0; i < 3; ++i) ok &= ld_agent(p.ws + WS_START + 8 * min(lane + 64 * i, NMAIN - 1)) == tag;
#pragma unroll
            for (int i = 0; i < 8; ++i) ok &= ((ld_agent(p.ws + WS_G + 32 * (lane + 64 * i)) ^ (tag << 16)) >> 16) == 0;
            if (__all(ok)) break;
            __builtin_amdgcn_s_sleep(8);
        }
        if (lane == 0) st_agent(p.ws, tag);
    }
}

__global__ void __launch_bounds__(NT, 3) ssm_mix_kernel(Params) {
    __shared__ __attribute__((aligned(16))) char smem[LDS_TOTAL];
    const int tid = threadIdx.x, lane = tid & 63;
    const int wv = __builtin_amdgcn_readfirstlane(tid >> 6);
    const int blk = blockIdx.x;
    if (blk >= NMAIN) {
        if (wv < 8) gate_block(blk - NMAIN);
        else {
            u64 pc; int dl;
            asm volatile("s_getpc_b64 %0\n.Lpc%=:\n\ts_mov_b32 %1, _Z14ssm_mix_kernel6Params-.Lpc%=" : "=s"(pc), "=s"(dl));
            const unsigned o = min(128u * (64 * (wv - 8) + lane), (unsigned)-dl);
            const unsigned a = *(const GLB1 unsigned*)(pc + (long long)dl + (o & ~3u));
            asm volatile("" :: "v"(a));
        }
        return;
    }
    const unsigned sb = (unsigned)(size_t)(LDS3 char*)smem;
    const int i0 = blk >> 3, grp = (blk & 7) * 20 + i0;

    if (wv >= 8) {
        const int k = wv - 8;
        const Head p = args_head();
        PRIO(3);
        if (k == 0 && lane < 20) ldsw<unsigned>(sb + FLG + 4 * lane, 0u);
        const auto rx = __builtin_amdgcn_make_buffer_rsrc((void*)p.x, 0, L * D * 2, 0x00020000);
        const int g0_ = grp - i0, lo_ = max(g0_, grp & ~15), cnt_ = min(g0_ + 20, (grp & ~15) + 16) - lo_;
        const int r0 = 64 * k + 64 * ((grp - lo_) * 32 / cnt_);
        asm volatile("s_waitcnt lgkmcnt(0)\n\ts_barrier" ::: "memory");
        u64 tag = read_tag(p.ws);
        u2 xv[8];
#pragma unroll
        for (int i = 0; i < 8; ++i) xv[i] = __builtin_amdgcn_raw_buffer_load_b64(rx, (((r0 + 256 * i) & (L - 1)) + lane) * (D * 2) + grp * 8, 0, 0);
        PIN();
        asm volatile("" : "+v"(tag));
        const unsigned raw = sb + RAW + 4096 * k;
        const unsigned ta = raw + 8 * ((lane & 48) + 4 * (lane & 3) + ((lane >> 2) & 3));
        const unsigned ua = sb + UO + (lane & 3) * U_C + 2 * (240 + (lane & 60));
#pragma unroll
        for (int i = 0; i < 9; ++i) {
            if (i < 8) {
                ldswv<u2>(raw + 512 * i + 8 * lane, xv[i]);
                asm volatile("" ::: "memory");
                xv[i] = ldstr(ta + 512 * i);
                asm volatile("" ::: "memory");
            }
            if (i > 0) ldswv<u2>(ua + 2 * ((r0 + 256 * (i - 1)) & (L - 1)), xv[i - 1]);
            PIN();
        }
        ldswv<unsigned char>(sb + FLG + 64 + k, 1);
        PRIO(0);
        __builtin_amdgcn_s_sleep(20);
        const char* gp = p.ws + WS_G + 8 * (64 * k + lane);
        const u64 want = tag << 16;
        u64 gr[8];
#pragma unroll 1
        for (unsigned spin = 0; spin < SPIN_CAP; ++spin) {
            u64 df = 0;
#pragma unroll
            for (int T = 0; T < 8; ++T) gr[T] = ld_agent(gp + 2048 * T);
#pragma unroll
            for (int T = 0; T < 8; ++T) df |= gr[T] ^ want;
            if (__builtin_amdgcn_ballot_w64(((unsigned)(df >> 32) | ((unsigned)df >> 16)) != 0) == 0) break;
            __builtin_amdgcn_s_sleep(8);
        }
        if (k == 0) {
            wait4(0, sb + FLG + 64);
            if (lane == 0) st_agent(p.ws + WS_START + 8 * blk, tag);
        }
        float g1[8], g0[8];
#pragma unroll
        for (int T = 0; T < 8; ++T) { g1[T] = bf2f((unsigned)gr[T]); g0[T] = rbf(1.f - g1[T]); asm volatile("" : "+v"(g1[T]), "+v"(g0[T])); }
        PRIO(3);
        const int rr = (lane >> 2) & 3;
        const unsigned oa = sb + ((rr >> 1) + 4 * (rr & 1)) * TB_W + 16 * rr + 8 * (4 * k + (lane >> 4) + 24 * (lane & 3));
        const auto ro = __builtin_amdgcn_make_buffer_rsrc((void*)p.out, 0, L * D * 2, 0x00020000);
        const int ov = (64 * k + lane) * (D * 2) + grp * 8;
        u2 nf = {0, 0};
#pragma unroll
        for (int T = 0; T < 8; ++T) {
            if (__builtin_amdgcn_readfirstlane(nf.x & nf.y) != 0x01010101u) {
#pragma unroll 1
                for (unsigned spin = 0; spin < SPIN_CAP; ++spin) {
                    const u2 fl = ldsrv<u2>(sb + FLG + 8 * T);
                    if (__builtin_amdgcn_readfirstlane(fl.x & fl.y) == 0x01010101u) break;
                    if (T < 6) __builtin_amdgcn_s_sleep(1);
                }
            }
            asm volatile("" ::: "memory");
            const u2 a = ldstr(oa + T * OS_T), b = ldstr(oa + T * OS_T + 2 * TB_W);
            asm volatile("" ::: "memory");
            if (T < 7) nf = ldsrv<u2>(sb + FLG + 8 * T + 8);
            const float o0 = mix(a.x, g1[T], g0[T]), o1 = mix(a.y, g1[T], g0[T]), o2 = mix(b.x, g1[T], g0[T]), o3 = mix(b.y, g1[T], g0[T]);
            __builtin_amdgcn_raw_buffer_store_b64(u2{pkbf(o0, o1), pkbf(o2, o3)}, ro, ov, T * 256 * D * 2, 0);
        }
        return;
    }

    const int c = wv & 3, s = wv >> 2, ch = grp * 4 + c;
    const Ssm q = args_ssm(s);
    PIN();
    PRIO(2);
    const unsigned tb = sb + wv * TB_W, ka = sb + KA + wv * KA_W, ub = sb + UO + c * U_C;
    const int n = lane & 15, h = lane >> 4;
    unsigned po = 2 * (ch * NM + 6 * (lane & 31)), co = 2 * (ch & ~1);
    asm volatile("" : "+v"(po), "+v"(co));

    PIN();
    u3 d0, d1, d2;
    unsigned ldt, dskw;
    asm volatile("global_load_dword %3, %6, %13\n\tglobal_load_dword %4, %6, %14\n\t"
                 "s_mov_b32 exec_hi, 0\n\tglobal_load_dwordx3 %0, %5, %7\n\t"
                 "s_not_b64 exec, exec\n\tglobal_load_dwordx3 %0, %5, %8\n\tglobal_load_dwordx3 %1, %5, %10\n\t"
                 "s_not_b64 exec, exec\n\tglobal_load_dwordx3 %1, %5, %9\n\tglobal_load_dwordx3 %2, %5, %11\n\t"
                 "s_not_b64 exec, exec\n\tglobal_load_dwordx3 %2, %5, %12\n\ts_mov_b64 exec, -1"
                 : "=&v"(d0), "=&v"(d1), "=&v"(d2), "=&v"(ldt), "=&v"(dskw)
                 : "v"(po), "v"(co), "s"(q.q[0]), "s"(q.q[1]), "s"(q.q[2]), "s"(q.q[3]), "s"(q.q[4]), "s"(q.q[5]), "s"(q.q[6]), "s"(q.q[7])
                 : "memory", "scc");
    PIN();
    if (wv < 6) ldsw<u4>(sb + BND + 1024 * wv + 16 * lane, band(lane, (0xc54b36 >> (4 * wv)) & 15, (0x279335 >> (4 * wv)) & 15, wv == 5 ? 0xffffu : 0x3f80u));
    else if (lane < 60) ldsw<u4>(sb + UO + (2 * (wv - 6) + (lane >= 30)) * U_C + 16 * (lane >= 30 ? lane - 30 : lane), u4{0, 0, 0, 0});
    if (lane < 52) ldsw<u4>(ka + T3O + 16 * lane, u4{0, 0, 0, 0});
    asm volatile("s_mov_b32 m0, %0\n\ts_nop 0" ::"s"(tb) : "memory");
    putrow<0>(h2{(_Float16)1.f, (_Float16)0.f});
    unsigned zra = tb + 16 * h, vra = zra + n * ROW;
    unsigned wra1 = zra + ((unsigned)(0x8765fedcba994321ull >> (4 * n)) & 15) * ROW;
    unsigned wra2 = zra + (((unsigned)(0x88883333dc33ba98ull >> (4 * n)) & 15) + 8) * ROW;
    asm volatile("" : "+v"(zra), "+v"(vra), "+v"(wra1), "+v"(wra2));
    unsigned ga = ub + 32 * n + 16 * h;
    unsigned ba = sb + BND + 16 * lane;
    unsigned t3a = ka + T3O + 4 * (95 - lane), t4a = ka + T4O + 2 * (95 - 64 * (lane >> 5) - (lane & 31)), xa = ka + 2 * 536 - 16 * lane;
    unsigned k3a = ka + T3O + 2 * (60 - 4 * n + 16 * h), k4a = ka + T4O + 4 * (15 - n + 4 * h);
    unsigned fa = ka + (n & 3) * 1216 + ((n & 3) == 3 ? 8 : 0) + 2 * (16 - 4 * (n >> 2) + 8 * h);
    unsigned oa = tb + 16 * (2 * (c & 1) + s) + 8 * (n + 24 * h), fl = sb + FLG + wv, one = 1;
    asm volatile("" : "+v"(ga), "+v"(ba), "+v"(t3a), "+v"(t4a), "+v"(xa), "+v"(k3a), "+v"(k4a), "+v"(fa), "+v"(oa), "+v"(fl), "+v"(one));
    PIN();
    asm volatile("s_waitcnt lgkmcnt(0)\n\ts_barrier" ::: "memory");
    PIN();
    const unsigned e_ = min((unsigned)(lane - 16), 39u), sg_ = __umul24(e_, 26u) >> 8, q_ = e_ - 10 * sg_;
    unsigned eda = sg_ * 1216 + (sg_ == 3 ? 8 : 0) + (q_ < 3 ? 1096 + 8 * q_ : 8 * min(q_ - 2, sg_ < 2 ? 6u : 7u));
    eda = wl(X0 + 1088, 0, wl(X1 + 1088, 1, wl(X2 + 1088, 2, wl(X0 + 56, 61, wl(X1 + 56, 62, wl(X3 + 64, 63, eda)))))) + ka;
    unsigned sel0 = wl(0x0c0c0302u, 0, wl(0x03020100u, 1, wl(0x07060504u, 2, wl(0x03020c0cu, 63, 0x0c0c0c0cu))));
    unsigned sel1 = wl(0x0c0c0302u, 2, wl(0x03020100u, 61, wl(0x03020c0cu, 62, wl(0x03020100u, 63, 0x0c0c0c0cu))));
    asm volatile("" : "+v"(eda), "+v"(sel0), "+v"(sel1));
    PIN();

    asm volatile("s_waitcnt vmcnt(6)" : "+v"(ldt), "+v"(dskw));
    const unsigned hs = (c & 1) ? 0 : 16;
    const float dt = exp_bf(bfhi(ldt << hs));
    float dsk = lane == 0 ? bfhi(dskw << hs) : 0.f;
    float ka2 = dt * (s ? -1.4426950408889634f : 1.4426950408889634f), kf = dt * 0.15915494309189535f, dts = s ? 2.f * dt : dt;
    asm volatile("" : "+v"(ka2), "+v"(kf), "+v"(dsk), "+v"(dts));
    PIN();

    float pa[3], pf[3], cr[3], ci[3], br[3], bi[3];
    asm volatile("s_waitcnt vmcnt(4)" : "+v"(d0));
    unsigned m0_[6] = {d0.x << 16, d0.x & 0xffff0000u, d0.y << 16, d0.y & 0xffff0000u, d0.z << 16, d0.z & 0xffff0000u}, m1[6], m2[6];
    h2 Z[3][11];
    f2 pc[3], zz, zt;
#define SQ(I, X, Y) PIN(); X; PIN(); zt = zz * zz.xx; Z[0][I] = toh(zz); PIN(); Y; PIN(); zz = cfin(zz, zz, zt);
#define SW(M, J, P, Q) { const auto r_ = __builtin_amdgcn_permlane32_swap(M[J], M[J + 3], false, false); P[J] = __uint_as_float(r_[0]); Q[J] = __uint_as_float(r_[1]); }
    SW(m0_, 0, pa, pf) SW(m0_, 1, pa, pf) SW(m0_, 2, pa, pf)
    if (s != 0) {
#pragma unroll
        for (int j = 0; j < 3; ++j) pa[j] = rbf(__builtin_amdgcn_exp2f(pa[j] * 1.4426950408889634f));
    }
    {
        pa[0] *= ka2; pf[0] *= kf;
        const float t1 = __builtin_amdgcn_fractf(pf[0]), e1 = __builtin_amdgcn_exp2f(pa[0]);
        zz = f2{__builtin_amdgcn_cosf(t1), __builtin_amdgcn_sinf(t1)};
        pa[1] *= ka2; pf[1] *= kf; pa[2] *= ka2; pf[2] *= kf;
        zz = zz * e1;
    }
    asm volatile("s_waitcnt vmcnt(0)" : "+v"(d1), "+v"(d2));
    SQ(0, m1[0] = d1.x << 16, m1[3] = d1.y & 0xffff0000u)
    SQ(1, m1[1] = d1.x & 0xffff0000u, m1[4] = d1.z << 16)
    SQ(2, m1[2] = d1.y << 16, m1[5] = d1.z & 0xffff0000u)
    SQ(3, m2[0] = d2.x << 16, m2[3] = d2.y & 0xffff0000u)
    SQ(4, m2[1] = d2.x & 0xffff0000u, m2[4] = d2.z << 16)
    SQ(5, m2[2] = d2.y << 16, m2[5] = d2.z & 0xffff0000u)
    SQ(6, SW(m1, 0, cr, ci), SW(m1, 1, cr, ci))
    SQ(7, SW(m1, 2, cr, ci), SW(m2, 0, br, bi))
    SQ(8, SW(m2, 1, br, bi), SW(m2, 2, br, bi))
    SQ(9, , )
#undef SQ
#undef SW
    PIN();
#pragma unroll
    for (int j = 0; j < 3; ++j) pc[j] = f2{cr[j], ci[j]};
    Z[0][10] = toh(zz);
    if (s == 0) {
#pragma unroll
        for (int j = 0; j < 3; ++j) { const f2 b = {br[j], bi[j]}; pc[j] = cfin(pc[j], b, pc[j] * b.xx); }
    }
    unsigned mi = __float_as_uint(max3abs(max3abs(pc[0].x, pc[0].y, pc[1].x), max3abs(pc[1].y, pc[2].x, pc[2].y), 0.f));
    mi = max(mi, (unsigned)__builtin_amdgcn_update_dpp(0, (int)mi, 0x128, 0xf, 0xf, false));
    mi = max(mi, (unsigned)__builtin_amdgcn_update_dpp(0, (int)mi, 0x124, 0xf, 0xf, false));
    mi = max(mi, (unsigned)__builtin_amdgcn_update_dpp(0, (int)mi, 0x122, 0xf, 0xf, false));
    mi = max(mi, (unsigned)__builtin_amdgcn_update_dpp(0, (int)mi, 0x121, 0xf, 0xf, false));
    const unsigned ms = max(max((unsigned)__builtin_amdgcn_readlane(mi, 0), (unsigned)__builtin_amdgcn_readlane(mi, 16)),
                            max((unsigned)__builtin_amdgcn_readlane(mi, 32), (unsigned)__builtin_amdgcn_readlane(mi, 48)));
    const unsigned eb = min(max(ms >> 23, 20u), 240u);
    const float scale = __uint_as_float((267u - eb) << 23);
    float inv = dts * __uint_as_float((eb - 13u) << 23);
    asm volatile("" : "+v"(inv));
    h2 C0[3];
#pragma unroll
    for (int j = 0; j < 3; ++j) C0[j] = toh(pc[j] * f2{scale, -scale});

    f4 kacc[4] = {};
    unsigned xfl;
    u4 vf[4], zf[4], hf[4];
    h8 wf[2][4];
#define KMM(T, Q) kacc[T] = __builtin_amdgcn_mfma_f32_16x16x32_f16(wf[T & 1][Q], __builtin_bit_cast(h8, T < 2 ? vf[Q] : hf[Q]), kacc[T], 0, 0, 0); PIN();
#define HMM(Q, E) hf[Q][E] = h2u(u2h(vf[Q][E]) * u2h(zf[Q][E]).xx); PIN();
#define HFF(Q, E) { unsigned r_; asm("v_pk_fma_f16 %0, %1, %2, %3 op_sel:[1,1,0] op_sel_hi:[0,1,1] neg_lo:[1,0,0]" : "=v"(r_) : "v"(vf[Q][E]), "v"(zf[Q][E]), "v"(hf[Q][E])); hf[Q][E] = r_; } PIN();
#define KM(T, Q) if (j > 0) { KMM(T, Q) }
#define HM(Q, E) if (j > 0) { HMM(Q, E) }
#define HF(Q, E) if (j > 0) { HFF(Q, E) }
#define VM(N, X, Y) V[N] = V[X] * V[Y].xx; PIN();
#define VF(N, X, Y) V[N] = __builtin_elementwise_fma(h2{-V[X].y, V[X].x}, V[Y].yy, V[N]); PIN();
#define CH0 if (j < 2) { t1 = __builtin_amdgcn_fractf(pf[j + 1]); e1 = __builtin_amdgcn_exp2f(pa[j + 1]); } PIN();
#define CH1 if (j < 2) { zz = f2{__builtin_amdgcn_cosf(t1), __builtin_amdgcn_sinf(t1)}; } PIN();
#define CH2 if (j < 2) { zz = zz * e1; } PIN();
#define CHA if (j < 2) { zt = zz * zz.xx; } PIN();
#define CHC(I) if (j < 2) { Z[j + 1][I] = toh(zz); } PIN();
#define CHB if (j < 2) { zz = cfin(zz, zz, zt); } PIN();
#define PM(N, X, Y) const h2 N##_t = X * Y.xx; PIN();
#define PF(N, X, Y) const h2 N = __builtin_elementwise_fma(h2{X.y, -X.x}, Y.yy, N##_t); PIN();
#define VW(R) putrow<R>(V[R]); PIN();
#pragma unroll
    for (int j = 0; j < 3; ++j) {
        if (j == 1) { PRIO(1); } if (j == 2) { PRIO(0); }
        const h2* z = Z[j];
        h2 V[17];
        float t1, e1;
        V[1] = z[3]; V[2] = z[4]; V[4] = z[5]; V[8] = z[6]; V[16] = z[7];
        VM(3, 2, 1) VM(12, 8, 4) VF(3, 2, 1) VM(5, 4, 1) VF(12, 8, 4) VM(6, 4, 2) VF(5, 4, 1) VM(7, 4, 3) VF(6, 4, 2) VM(9, 8, 1) VF(7, 4, 3)
        VM(10, 8, 2) VF(9, 8, 1) VM(11, 8, 3) VF(10, 8, 2) VM(13, 12, 1) VF(11, 8, 3) VM(14, 12, 2) VF(13, 12, 1) VM(15, 12, 3) VF(14, 12, 2)
        VF(15, 12, 3)
        const h2 W0 = C0[j];
        CH0 PM(W1, W0, z[0]) PM(W2, W0, z[1]) CH1 PF(W1, W0, z[0]) PM(W4, W0, z[2]) CH2 PF(W2, W0, z[1]) PM(W3, W1, z[1]) CHA CHC(0) PF(W4, W0, z[2])
        PM(W8, W0, z[3]) CHB PF(W3, W1, z[1]) PM(W5, W1, z[2]) CHA CHC(1) PF(W8, W0, z[3]) PM(W6, W2, z[2]) CHB PF(W5, W1, z[2]) PM(W7, W3, z[2]) CHA
        CHC(2) PF(W6, W2, z[2]) PM(r256, W0, z[8]) CHB PF(W7, W3, z[2]) PM(r258, W2, z[8]) CHA CHC(3) PF(r256, W0, z[8]) PM(r260, W4, z[8]) CHB
        PF(r258, W2, z[8]) PM(r262, W6, z[8]) CHA CHC(4) PF(r260, W4, z[8]) PM(r264, W8, z[8]) CHB PF(r262, W6, z[8]) PM(r516, W4, z[9]) CHA CHC(5)
        PF(r264, W8, z[8]) PM(r520, W8, z[9]) CHB PF(r516, W4, z[9]) PM(r1032, W8, z[10]) CHA CHC(6) PF(r520, W8, z[9]) PM(r772, r516, z[8]) CHB
        PF(r1032, W8, z[10]) PM(r1544, r1032, z[9]) CHA CHC(7) PF(r772, r516, z[8]) PM(r776, r520, z[8]) CHB PF(r1544, r1032, z[9])
        PM(r1288, r1032, z[8]) CHA CHC(8) PF(r776, r520, z[8]) PM(r1800, r1544, z[8]) CHB PF(r1288, r1032, z[8]) CHA CHC(9) CHB CHC(10)
        PF(r1800, r1544, z[8])
        KM(0, 0) HM(0, 0) HM(0, 1) KM(1, 0) HM(0, 2) HM(0, 3) HF(0, 0) HF(0, 1) HF(0, 2) HF(0, 3) KM(0, 1) HM(1, 0) HM(1, 1) KM(1, 1) HM(1, 2)
        HM(1, 3) KM(2, 0) HF(1, 0) HF(1, 1) KM(3, 0) HF(1, 2) HF(1, 3) KM(0, 2) HM(2, 0) HM(2, 1) KM(1, 2) HM(2, 2) HM(2, 3) KM(2, 1) HF(2, 0)
        HF(2, 1) KM(3, 1) HF(2, 2) HF(2, 3) KM(0, 3) HM(3, 0) HM(3, 1) KM(1, 3) HM(3, 2) HM(3, 3) KM(2, 2) HF(3, 0) HF(3, 1) KM(3, 2) HF(3, 2)
        HF(3, 3) VW(1) VW(2) KM(2, 3) KM(3, 3)
        VW(3) VW(4) VW(5) VW(6) VW(7) VW(8) VW(9) VW(10) VW(11) VW(12) VW(13) VW(14) VW(15) VW(16)
#pragma unroll
        for (int q = 0; q < 4; ++q) { vf[q] = ldsr<u4>(vra + 64 * q); zf[q] = ldsr<u4>(zra + 16 * ROW + 64 * q); }
        if (j == 2) xfl = ldsrv<unsigned>(sb + FLG + 64);
        putrow<1>(W0); putrow<2>(W1); putrow<3>(W2); putrow<4>(W3); putrow<5>(r772); putrow<6>(r776); putrow<7>(r1800); putrow<8>(r1544);
        putrow<9>(r256); putrow<10>(r258); putrow<11>(r260); putrow<12>(r516); putrow<13>(r520); putrow<14>(r1288); putrow<15>(r1032); putrow<16>(W4);
        putrow<17>(W5); putrow<18>(W6); putrow<19>(W7); putrow<20>(r262); putrow<21>(r264);
#pragma unroll
        for (int q = 0; q < 4; ++q) { wf[0][q] = ldsr<h8>(wra1 + 64 * q); wf[1][q] = ldsr<h8>(wra2 + 64 * q); }
        PIN();
    }
    if (s == 1) PRIO(3);
    wait4(xfl, sb + FLG + 64);
    b8 A16[6], A24[6], G[64], B9, B7, B5, B3a, B3b;
    u4 msp;
    A24[0] = ldsr<b8>(ga + 64 * 7 - 16); A16[0] = ldsr<b8>(ga + 64 * 7);
    B9 = ldsr<b8>(ba + 3072); B7 = ldsr<b8>(ba + 4096); B5 = ldsr<b8>(ba); B3a = ldsr<b8>(ba + 1024); B3b = ldsr<b8>(ba + 2048);
    msp = ldsr<u4>(ba + 5120);
    PIN();
    KMM(0, 0) HMM(0, 0) HMM(0, 1) KMM(1, 0) HMM(0, 2) HMM(0, 3) HFF(0, 0) HFF(0, 1) HFF(0, 2) HFF(0, 3) KMM(0, 1) HMM(1, 0) HMM(1, 1) KMM(1, 1)
    HMM(1, 2) HMM(1, 3) KMM(2, 0) HFF(1, 0) HFF(1, 1) KMM(3, 0) HFF(1, 2) HFF(1, 3) KMM(0, 2) HMM(2, 0) HMM(2, 1) KMM(1, 2) HMM(2, 2) HMM(2, 3)
    KMM(2, 1) HFF(2, 0) HFF(2, 1) KMM(3, 1) HFF(2, 2) HFF(2, 3) KMM(0, 3) HMM(3, 0) HMM(3, 1) KMM(1, 3) HMM(3, 2) HMM(3, 3) KMM(2, 2) HFF(3, 0)
    HFF(3, 1) KMM(3, 2) HFF(3, 2) HFF(3, 3)
    asm volatile("s_nop 1");
    PIN();
    KMM(2, 3) KMM(3, 3)
#undef KMM
#undef HMM
#undef HFF
#undef KM
#undef HM
#undef HF
#undef VM
#undef VF
#undef CH0
#undef CH1
#undef CH2
#undef CHA
#undef CHB
#undef CHC
#undef PM
#undef PF
#undef VW
    if (s == 0) PRIO(2);

    unsigned Dn[4], Df[4], E0, E1, E2, E3, E4, D4, KK, k1024;
    unsigned l0, h0, l1, h1;
    auto pack = [&](int t, int part) {
        const f4 lo = kacc[t], hi = kacc[2 + t];
        if (part == 0) { l0 = pkbf(lo[1] * inv, __builtin_fmaf(lo[0], inv, t == 0 ? dsk : 0.f)); }
        if (part == 1) { h0 = pkbf(smul(hi[1], inv), hi[0] * inv); }
        if (part == 2) { l1 = pkbf(lo[3] * inv, smul(lo[2], inv)); }
        if (part == 3) { h1 = pkbf(smul(hi[3], inv), hi[2] * inv); }
        if (part == 4) {
            const auto r0 = __builtin_amdgcn_permlane16_swap(l0, h0, false, false);
            const auto r1 = __builtin_amdgcn_permlane16_swap(l1, h1, false, false);
            l0 = r0[0]; h0 = r0[1]; l1 = r1[0]; h1 = r1[1];
        }
        if (part == 5) {
            const auto t0 = __builtin_amdgcn_permlane32_swap(l0, h0, false, false);
            const auto t1 = __builtin_amdgcn_permlane32_swap(l1, h1, false, false);
            Dn[2 * t] = t0[0]; Df[2 * t] = t0[1]; Dn[2 * t + 1] = t1[0]; Df[2 * t + 1] = t1[1];
        }
    };
    auto far_tables = [&]() {
        ldswv<unsigned>(t3a, Df[0]);
        ldswv<u16>(t4a + 64, (u16)Df[1]);
        ldswv<u16>(t4a, (u16)(Df[1] >> 16));
        k1024 = ((unsigned)__builtin_amdgcn_readlane(Df[0], 63) & 0xffffu) * 0x10001u;
        asm volatile("" ::: "memory");
    };
    auto near_tables = [&](int part) {
        if (part == 0) {
            E0 = __builtin_amdgcn_alignbit(Dn[0], Dn[1], 16); E1 = __builtin_amdgcn_alignbit(Dn[1], Dn[2], 16); E2 = __builtin_amdgcn_alignbit(Dn[2], Dn[3], 16);
            KK = __builtin_amdgcn_perm(Dn[3], Dn[3], 0x01000100u);
        }
        if (part == 6) {
            D4 = __builtin_amdgcn_update_dpp(KK, Dn[0], 0x130, 0xf, 0xf, false);
            E4 = __builtin_amdgcn_update_dpp(KK, E0, 0x130, 0xf, 0xf, false);
            E3 = __builtin_amdgcn_alignbit(Dn[3], D4, 16);
        }
        if (part == 1) ldswv<u4>(xa + X2, u4{E4, E3, E2, E1});
        if (part == 2) ldswv<u4>(xa + X1, u4{D4, Dn[3], Dn[2], Dn[1]});
        if (part == 3) ldswv<u4>(xa + X0, u4{E3, E2, E1, E0});
        if (part == 4) ldswv<u4>(xa + X3 + 8, u4{Dn[3], Dn[2], Dn[1], Dn[0]});
        if (part == 5) {
            unsigned S0 = __builtin_amdgcn_update_dpp(0u, Dn[0], 0x150, 0x1, 0xf, false);
            S0 = __builtin_amdgcn_update_dpp(S0, KK, 0x15f, 0x8, 0xf, false);
            const unsigned S1 = __builtin_amdgcn_update_dpp(0u, E0, 0x150, 0x1, 0xf, false);
            ldswv<u2>(eda, u2{__builtin_amdgcn_perm(S1, S0, sel0), __builtin_amdgcn_perm(S1, S0, sel1)});
        }
    };
    const f4 z = {0.f, 0.f, 0.f, 0.f};
    f4 acc[8] = {}, R[2][6];
    b8 F[17], K3[3][2], K4[4], Fs;
    u4 Z4, Z3[2];
    u2 pk;
#define BOX(I, J, A, B) R[I & 1][J] = mma(A[I], B, z);
#define CV4(I, E) Z4[E] = pkbf(R[I & 1][4][E], R[I & 1][5][E]);
#define CV3(I, H, E) Z3[H][E] = pkbf(R[I & 1][2 * (E & 1)][2 * H + (E >> 1)], R[I & 1][2 * (E & 1) + 1][2 * H + (E >> 1)]);
#define SPC(I) acc[I + 4] = mma(Fs, A16[I], acc[I + 4]);
#define FA4(I, Q) acc[I + 4 + Q] = mma(b8c(Z4), K4[Q], acc[I + 4 + Q]);
#define FA3(I, H, Q) acc[I + 2 + Q] = mma(b8c(Z3[H]), K3[Q][H], acc[I + 2 + Q]);
#define NR(T, t) acc[T] = mma(F[t], G[8 * T - t + 7], acc[T]);
#define LA(I) A24[I] = ldsr<b8>(ga + 64 * (8 * I + 7) - 16); A16[I] = ldsr<b8>(ga + 64 * (8 * I + 7));
#define LF(t) F[t] = frag64(fa + 64 * (16 - t));
#define LG(w) G[w] = ldsr<b8>(ga + 64 * w);
#define LK4(Q) K4[Q] = frag32(k4a + 64 * (3 - Q));
#define LK3(Q) K3[Q][0] = frag64(k3a + 128 * (2 - Q)); K3[Q][1] = frag64(k3a + 128 * (2 - Q) + 16);
#define FN0(T) pk = u2{pkbf(acc[T][0], acc[T][1]), pkbf(acc[T][2], acc[T][3])};
#define FN1(T) ldswv<u2>(oa + T * OS_T, pk);
#define FN2(T) ldswv<unsigned char>(fl + 8 * T, (unsigned char)one);
    LA(1) BOX(0, 4, A24, B9) pack(0, 0); pack(0, 1); PIN();
    BOX(0, 5, A16, B7) pack(0, 2); pack(0, 3); PIN();
    BOX(0, 0, A24, B5) pack(0, 4); PIN();
    BOX(0, 1, A16, B3a) pack(0, 5); PIN();
    BOX(0, 2, A16, B5) far_tables(); PIN();
    BOX(0, 3, A16, B3b) LK4(3) LK4(2) PIN();
    BOX(1, 4, A24, B9) LK4(1) LK4(0) PIN();
    BOX(1, 5, A16, B7) LK3(0) CV4(0, 0) CV4(0, 1) PIN();
    BOX(1, 0, A24, B5) LK3(1) CV4(0, 2) CV4(0, 3) PIN();
    BOX(1, 1, A16, B3a) LK3(2) CV3(0, 0, 0) CV3(0, 0, 1) PIN();
    BOX(1, 2, A16, B5) CV3(0, 0, 2) CV3(0, 0, 3) CV3(0, 1, 0) PIN();
    BOX(1, 3, A16, B3b) CV3(0, 1, 1) CV3(0, 1, 2) CV3(0, 1, 3) PIN();
    Fs = b8c(msp & k1024); PIN();
    FA4(0, 3) LA(2) PIN();
    FA4(0, 2) pack(1, 0); PIN();
    FA4(0, 1) pack(1, 1); PIN();
    FA4(0, 0) pack(1, 2); PIN();
    FA3(0, 0, 0) pack(1, 3); PIN();
    FA3(0, 0, 1) pack(1, 4); PIN();
    FA3(0, 0, 2) pack(1, 5); PIN();
    FA3(0, 1, 0) near_tables(0); PIN();
    FA3(0, 1, 1) near_tables(6); PIN();
    FA3(0, 1, 2) near_tables(1); PIN();
    SPC(0) PIN();
    BOX(2, 4, A24, B9) CV4(1, 0) CV4(1, 1) PIN();
    BOX(2, 5, A16, B7) CV4(1, 2) CV4(1, 3) PIN();
    BOX(2, 0, A24, B5) CV3(1, 0, 0) CV3(1, 0, 1) PIN();
    BOX(2, 1, A16, B3a) CV3(1, 0, 2) CV3(1, 0, 3) PIN();
    BOX(2, 2, A16, B5) CV3(1, 1, 0) CV3(1, 1, 1) PIN();
    BOX(2, 3, A16, B3b) CV3(1, 1, 2) CV3(1, 1, 3) PIN();
    SPC(1) LA(3) PIN();
    FA4(1, 2) near_tables(2); PIN();
    FA4(1, 1) PIN();
    FA4(1, 0) near_tables(3); PIN();
    FA3(1, 0, 0) PIN();
    FA3(1, 0, 1) near_tables(4); PIN();
    FA3(1, 0, 2) PIN();
    FA3(1, 1, 0) near_tables(5); PIN();
    FA3(1, 1, 1) PIN();
    FA3(1, 1, 2) PIN();
    BOX(3, 4, A24, B9) CV4(2, 0) PIN();
    BOX(3, 5, A16, B7) CV4(2, 1) PIN();
    BOX(3, 0, A24, B5) CV4(2, 2) PIN();
    BOX(3, 1, A16, B3a) CV4(2, 3) PIN();
    BOX(3, 2, A16, B5) CV3(2, 0, 0) PIN();
    BOX(3, 3, A16, B3b) CV3(2, 0, 1) PIN();
    SPC(2) CV3(2, 0, 2) LA(4) PIN();
    FA4(2, 1) CV3(2, 0, 3) PIN();
    FA4(2, 0) CV3(2, 1, 0) PIN();
    FA3(2, 0, 0) CV3(2, 1, 1) PIN();
    FA3(2, 0, 1) CV3(2, 1, 2) PIN();
    FA3(2, 0, 2) CV3(2, 1, 3) LF(7) PIN();
    FA3(2, 1, 0) LF(6) PIN();
    FA3(2, 1, 1) LF(5) PIN();
    FA3(2, 1, 2) LF(4) PIN();
    BOX(4, 0, A24, B5) CV4(3, 0) CV4(3, 1) PIN();
    BOX(4, 1, A16, B3a) CV4(3, 2) CV4(3, 3) PIN();
    BOX(4, 2, A16, B5) CV3(3, 0, 0) CV3(3, 0, 1) PIN();
    BOX(4, 3, A16, B3b) CV3(3, 0, 2) PIN();
    SPC(3) CV3(3, 0, 3) LA(5) PIN();
    FA4(3, 0) CV3(3, 1, 0) PIN();
    FA3(3, 0, 0) CV3(3, 1, 1) PIN();
    FA3(3, 0, 1) CV3(3, 1, 2) PIN();
    FA3(3, 0, 2) CV3(3, 1, 3) LF(3) PIN();
    FA3(3, 1, 0) LF(2) PIN();
    FA3(3, 1, 1) LF(1) PIN();
    FA3(3, 1, 2) LF(0) PIN();
    BOX(5, 0, A24, B5) CV3(4, 0, 0) CV3(4, 0, 1) PIN();
    BOX(5, 1, A16, B3a) CV3(4, 0, 2) CV3(4, 0, 3) PIN();
    BOX(5, 2, A16, B5) CV3(4, 1, 0) PIN();
    BOX(5, 3, A16, B3b) CV3(4, 1, 1) PIN();
    FA3(4, 0, 0) CV3(4, 1, 2) LG(0) PIN();
    FA3(4, 0, 1) CV3(4, 1, 3) LG(1) PIN();
    FA3(4, 1, 0) LG(2) PIN();
    FA3(4, 1, 1) LG(3) PIN();
    NR(0, 7) LG(4) LF(15) CV3(5, 0, 0) CV3(5, 0, 1) PIN();
    NR(0, 6) LG(5) LF(14) CV3(5, 0, 2) CV3(5, 0, 3) PIN();
    NR(0, 5) LG(6) LF(13) CV3(5, 1, 0) CV3(5, 1, 1) PIN();
    NR(0, 4) LG(7) LF(12) CV3(5, 1, 2) PIN();
    FA3(5, 0, 0) CV3(5, 1, 3) PIN();
    FA3(5, 1, 0) PIN();
    PRIO(2);
    NR(0, 3) LF(11) PIN();
    NR(0, 2) LF(10) PIN();
    NR(0, 1) LF(9) PIN();
    NR(0, 0) LF(8) LF(16) PIN();
    NR(1, 15) LG(8) PIN();
    NR(1, 14) LG(9) PIN();
    NR(1, 13) LG(10) PIN();
    NR(1, 12) LG(11) PIN();
    NR(1, 11) LG(12) FN0(0) PIN();
    NR(1, 10) LG(13) FN1(0) PIN();
    NR(1, 9) LG(14) PIN();
    NR(1, 8) LG(15) FN2(0) PIN();
    if (s == 0) PRIO(1);
    NR(1, 7) PIN();
    NR(1, 6) PIN();
    NR(1, 5) PIN();
    NR(1, 4) PIN();
    NR(1, 3) PIN();
    NR(1, 2) PIN();
    NR(1, 1) PIN();
    NR(1, 0) PIN();
    NR(2, 16) LG(16) PIN();
    NR(2, 15) LG(17) PIN();
    NR(2, 14) LG(18) PIN();
    NR(2, 13) LG(19) PIN();
    NR(2, 12) LG(20) FN0(1) PIN();
    NR(2, 11) LG(21) FN1(1) PIN();
    NR(2, 10) LG(22) PIN();
    NR(2, 9) LG(23) FN2(1) PIN();
    NR(2, 8) PIN();
    NR(2, 7) PIN();
    NR(2, 6) PIN();
    NR(2, 5) PIN();
    NR(2, 4) PIN();
    NR(2, 3) PIN();
    NR(2, 2) PIN();
    NR(2, 1) PIN();
    NR(2, 0) PIN();
    NR(3, 16) LG(24) PIN();
    NR(3, 15) LG(25) PIN();
    NR(3, 14) LG(26) PIN();
    NR(3, 13) LG(27) PIN();
    NR(3, 12) LG(28) FN0(2) PIN();
    NR(3, 11) LG(29) FN1(2) PIN();
    NR(3, 10) LG(30) PIN();
    NR(3, 9) LG(31) FN2(2) PIN();
    if (s == 1) PRIO(1);
    NR(3, 8) PIN();
    NR(3, 7) PIN();
    NR(3, 6) PIN();
    NR(3, 5) PIN();
    NR(3, 4) PIN();
    NR(3, 3) PIN();
    NR(3, 2) PIN();
    NR(3, 1) PIN();
    NR(3, 0) PIN();
    NR(4, 16) LG(32) PIN();
    NR(4, 15) LG(33) PIN();
    NR(4, 14) LG(34) PIN();
    NR(4, 13) LG(35) PIN();
    NR(4, 12) LG(36) FN0(3) PIN();
    NR(4, 11) LG(37) FN1(3) PIN();
    NR(4, 10) LG(38) PIN();
    NR(4, 9) LG(39) FN2(3) PIN();
    NR(4, 8) PIN();
    NR(4, 7) PIN();
    NR(4, 6) PIN();
    NR(4, 5) PIN();
    NR(4, 4) PIN();
    NR(4, 3) PIN();
    NR(4, 2) PIN();
    NR(4, 1) PIN();
    NR(4, 0) PIN();
    NR(5, 16) LG(40) PIN();
    NR(5, 15) LG(41) PIN();
    NR(5, 14) LG(42) PIN();
    NR(5, 13) LG(43) PIN();
    NR(5, 12) LG(44) FN0(4) PIN();
    NR(5, 11) LG(45) FN1(4) PIN();
    NR(5, 10) LG(46) PIN();
    NR(5, 9) LG(47) FN2(4) PIN();
    if (s == 0) PRIO(0);
    NR(5, 8) PIN();
    NR(5, 7) PIN();
    NR(5, 6) PIN();
    NR(5, 5) PIN();
    NR(5, 4) PIN();
    NR(5, 3) PIN();
    NR(5, 2) PIN();
    NR(5, 1) PIN();
    NR(5, 0) PIN();
    NR(6, 16) LG(48) PIN();
    NR(6, 15) LG(49) PIN();
    NR(6, 14) LG(50) PIN();
    NR(6, 13) LG(51) PIN();
    NR(6, 12) LG(52) FN0(5) PIN();
    NR(6, 11) LG(53) FN1(5) PIN();
    NR(6, 10) LG(54) PIN();
    NR(6, 9) LG(55) FN2(5) PIN();
    if (s == 1) PRIO(0);
    NR(6, 8) PIN();
    NR(6, 7) PIN();
    NR(6, 6) PIN();
    NR(6, 5) PIN();
    NR(6, 4) PIN();
    NR(6, 3) PIN();
    NR(6, 2) PIN();
    NR(6, 1) PIN();
    NR(6, 0) PIN();
    NR(7, 16) LG(56) PIN();
    NR(7, 15) LG(57) PIN();
    NR(7, 14) LG(58) PIN();
    NR(7, 13) LG(59) PIN();
    NR(7, 12) LG(60) FN0(6) PIN();
    NR(7, 11) LG(61) FN1(6) PIN();
    NR(7, 10) LG(62) PIN();
    NR(7, 9) LG(63) FN2(6) PIN();
    NR(7, 8) PIN();
    NR(7, 7) PIN();
    NR(7, 6) PIN();
    NR(7, 5) PIN();
    NR(7, 4) PIN();
    NR(7, 3) PIN();
    NR(7, 2) PIN();
    NR(7, 1) PIN();
    NR(7, 0) PIN();
    FN0(7) FN1(7) FN2(7)
}

extern "C" void kernel_launch(void* const* d_in, const int* in_sizes, int n_in, void* d_out, int out_size, void* d_ws, size_t ws_size,
                              hipStream_t stream) {
    Params p;
    p.x = (const u16*)d_in[0];
    p.gw = (const u16*)d_in[16];
    p.out = (u16*)d_out;
    p.ws = (char*)d_ws;
    const int idx[2][8] = {{1, 2, 5, 6, 3, 4, 7, 8}, {10, 11, 12, 13, 12, 13, 14, 15}};
    for (int s = 0; s < 2; ++s)
        for (int i = 0; i < 8; ++i) p.q[s][i] = (const u16*)d_in[idx[s][i]];
    ssm_mix_kernel<<<NWG, NT, 0, stream>>>(p);
}
```
